```python
import torch

def _softmax(x, axis=-1):
    return torch.softmax(x, dim=axis)
def _gelu(x):
    return torch.nn.functional.gelu(x, approximate="tanh")
def _silu(x):
    return torch.nn.functional.silu(x)
def _sigmoid(x):
    return torch.sigmoid(x)
def _layernorm(x, axis=-1, eps=1e-5):
    m = x.mean(dim=axis, keepdim=True)
    v = x.var(dim=axis, keepdim=True, unbiased=False)
    return (x - m) / torch.sqrt(v + eps)
def _scatter_add(tgt, idx, src, axis=0):
    return tgt.clone().index_add_(axis, idx, src)
def _segment_sum(src, seg, n):
    out = torch.zeros((n,) + src.shape[1:], dtype=src.dtype)
    return out.index_add_(0, seg, src)
def _pad(x, pw):
    flat = tuple(v for pair in reversed(pw) for v in pair)
    return torch.nn.functional.pad(x, flat)

kernel_name = 'standalone__adm_resblock__diffusion'

def setup_inputs():

    _w = (lambda a: (lambda t: t.to(torch.bfloat16) if t.is_floating_point() else t)(torch.as_tensor(a)))
    B, C, HW, E = 2, 128, 128, 768
    sc = (9.0 * C) ** -0.5
    return {
        "x": _w((lambda *s: torch.randn(*s, dtype=torch.bfloat16) * 0.05)(B, C, HW, HW)),
        "t_emb": _w((lambda *s: torch.randn(*s, dtype=torch.bfloat16) * 0.4)(B, E)),
        "g1": _w(1.0 + 0.1 * (lambda *s: torch.randn(*s, dtype=torch.bfloat16) * 0.4)(C)),
        "be1": _w(0.1 * (lambda *s: torch.randn(*s, dtype=torch.bfloat16) * 0.4)(C)),
        "w1": _w((lambda *s: torch.randn(*s, dtype=torch.bfloat16) * 0.4)(C, C, 3, 3) * sc),
        "w_emb": _w((lambda *s: torch.randn(*s, dtype=torch.bfloat16) * 0.4)(E, C) * (E ** -0.5)),
        "g2": _w(1.0 + 0.1 * (lambda *s: torch.randn(*s, dtype=torch.bfloat16) * 0.4)(C)),
        "be2": _w(0.1 * (lambda *s: torch.randn(*s, dtype=torch.bfloat16) * 0.4)(C)),
        "w2": _w((lambda *s: torch.randn(*s, dtype=torch.bfloat16) * 0.4)(C, C, 3, 3) * sc),
    }

def reference(x, t_emb, g1, be1, w1, w_emb, g2, be2, w2):
    B, C, H, W = x.shape
    G, eps = 4, 1e-5
    def gn(z, gamma, beta):
        zr = z.reshape(B, G, (C // G) * H * W)
        mu = zr.mean(-1).reshape(B, G, 1)
        var = ((zr - mu) ** 2).mean(-1).reshape(B, G, 1)
        zn = ((zr - mu) / torch.sqrt(var + eps)).reshape(B, C, H, W)
        return zn * gamma.reshape(1, C, 1, 1) + beta.reshape(1, C, 1, 1)
    def conv3(z, w):
        zp = _pad(z, ((0, 0), (0, 0), (1, 1), (1, 1)))
        taps = [torch.einsum("bchw,oc->bohw", zp[:, :, i:i+H, j:j+W], w[:, :, i, j]) for i in range(3) for j in range(3)]
        out = taps[0]
        for t in taps[1:]:
            out = out + t
        return out
    h = conv3(_silu(gn(x, g1, be1)), w1)
    emb = _silu(t_emb) @ w_emb
    h = h + emb.reshape(B, C, 1, 1)
    h = conv3(_silu(gn(h, g2, be2)), w2)
    return x + h
```

```cpp
#include <hip/hip_runtime.h>
#include <hip/hip_bf16.h>
typedef unsigned short u16;
__device__ __forceinline__ float bf2f(u16 v){ return __uint_as_float(((unsigned)v)<<16); }
__device__ __forceinline__ u16 f2bf(float f){ unsigned u=__float_as_uint(f); u = (u + 0x7FFF + ((u>>16)&1))>>16; return (u16)u; }
constexpr int B=2,C=128,H=128,W=128,E=768,G=4;
__global__ void stats_k(const u16* z, float* st){
  int bg=blockIdx.x; const u16* p=z+(size_t)bg*32*H*W;
  float s=0,q=0; for(int i=threadIdx.x;i<32*H*W;i+=blockDim.x){ float v=bf2f(p[i]); s+=v; q+=v*v; }
  __shared__ float ss[1024],qq[1024]; ss[threadIdx.x]=s; qq[threadIdx.x]=q; __syncthreads();
  for(int o=512;o>0;o>>=1){ if(threadIdx.x<o){ss[threadIdx.x]+=ss[threadIdx.x+o]; qq[threadIdx.x]+=qq[threadIdx.x+o];} __syncthreads(); }
  if(threadIdx.x==0){ float n=32.f*H*W; float m=ss[0]/n; float v=qq[0]/n-m*m; st[bg*2]=m; st[bg*2+1]=rsqrtf(v+1e-5f);} }
__global__ void act_k(const u16* z,const float* st,const u16* g,const u16* be,u16* o){
  int i=blockIdx.x*blockDim.x+threadIdx.x; int c=(i/(H*W))%C; int b=i/(C*H*W); int bg=b*G+c/32;
  float y=(bf2f(z[i])-st[bg*2])*st[bg*2+1]*bf2f(g[c])+bf2f(be[c]); y=y/(1.f+__expf(-y)); o[i]=f2bf(y);}
__global__ void emb_k(const u16* t,const u16* w,float* e){ int b=blockIdx.x,o=threadIdx.x; float s=0; for(int k=0;k<E;k++){ float v=bf2f(t[b*E+k]); v=v/(1.f+__expf(-v)); v=bf2f(f2bf(v)); s+=v*bf2f(w[k*C+o]);} e[b*C+o]=s; }
__global__ void conv_k(const u16* a,const u16* w,const float* e,const u16* res,u16* o){
  int i=blockIdx.x*blockDim.x+threadIdx.x; int x=i%W,y=(i/W)%H,oc=(i/(H*W))%C,b=i/(C*H*W);
  float s=0; for(int c=0;c<C;c++) for(int dy=0;dy<3;dy++) for(int dx=0;dx<3;dx++){ int yy=y+dy-1,xx=x+dx-1; if(yy<0||yy>=H||xx<0||xx>=W) continue; s+=bf2f(a[((b*C+c)*H+yy)*W+xx])*bf2f(w[(oc*C+c)*9+dy*3+dx]); }
  if(e) s+=e[b*C+oc]; if(res) s+=bf2f(res[i]); o[i]=f2bf(s);}
extern "C" void kernel_launch(void* const* d_in, const int* in_sizes, int n_in, void* d_out, int out_size, void* d_ws, size_t ws_size, hipStream_t stream) {
  const u16* x=(const u16*)d_in[0]; const u16* te=(const u16*)d_in[1]; const u16* g1=(const u16*)d_in[2]; const u16* be1=(const u16*)d_in[3];
  const u16* w1=(const u16*)d_in[4]; const u16* we=(const u16*)d_in[5]; const u16* g2=(const u16*)d_in[6]; const u16* be2=(const u16*)d_in[7]; const u16* w2=(const u16*)d_in[8];
  char* ws=(char*)d_ws; float* st=(float*)ws; float* e=(float*)(ws+4096); u16* a=(u16*)(ws+(1<<20)); u16* h=(u16*)(ws+(16<<20));
  int n=B*C*H*W;
  stats_k<<<8,1024,0,stream>>>(x,st); act_k<<<n/256,256,0,stream>>>(x,st,g1,be1,a); emb_k<<<2,128,0,stream>>>(te,we,e);
  conv_k<<<n/256,256,0,stream>>>(a,w1,e,nullptr,h); stats_k<<<8,1024,0,stream>>>(h,st); act_k<<<n/256,256,0,stream>>>(h,st,g2,be2,a);
  conv_k<<<n/256,256,0,stream>>>(a,w2,nullptr,x,(u16*)d_out);
}
```

```cpp
#include <hip/hip_runtime.h>
#include <stdint.h>

#define PRIO_C 0
#define PRIO_P 1

namespace {
typedef unsigned short u16;
typedef unsigned v4u __attribute__((ext_vector_type(4)));
typedef unsigned v3u __attribute__((ext_vector_type(3), aligned(4)));
typedef unsigned v2u __attribute__((ext_vector_type(2)));
typedef float f32x16 __attribute__((ext_vector_type(16)));
typedef float f32x4 __attribute__((ext_vector_type(4)));
typedef float f32x2 __attribute__((ext_vector_type(2)));
typedef __bf16 bf16x8 __attribute__((ext_vector_type(8)));
typedef __bf16 bf16x2 __attribute__((ext_vector_type(2)));
#define LAS __attribute__((address_space(3)))
#define DI __device__ __forceinline__

constexpr int CSV = 144, PLV = 34 * CSV;
constexpr int CSR = 112, PLR = 34 * CSR;
constexpr int L_V = 0;
constexpr int L_STG = 0;
constexpr int L_XK = 0, XKR = 8192 + 32;
constexpr int L_RAW = 16 * PLV;
constexpr int L_GB = L_RAW + 16 * PLR;
constexpr int L_EMB = L_GB + 2048;
constexpr int L_PART = L_EMB + 512;
constexpr int L_RED = L_PART + 256;
constexpr int L_AB = L_RED + 256;
constexpr int L_WT = L_AB + 4096;
constexpr int LDS_BYTES = L_WT + 6144;

constexpr int WS_REC1 = 4096, WS_REC2 = WS_REC1 + 16384, WS_EMB = WS_REC2 + 16384;
constexpr int WS_WT = 65536, WT_WAVE = 8 * 12 * 1024, WT_CONV = 4 * WT_WAVE, WT_COPY = 2 * WT_CONV;
constexpr int WS_HALO = WS_WT + 2 * WT_COPY, HALO_WG = 24576;
constexpr int WS_DBG = WS_HALO + 256 * HALO_WG;
constexpr int WS_BYTES = WS_DBG + 2 * 256 * 64 * 8 + 256 * 64 + 256 * 256 + 256 * 64;

constexpr int X_BYTES = 2 * 128 * 128 * 128 * 2;
constexpr int RSRC3 = 0x00020000;
constexpr int SC1 = 16;
constexpr int OOB = 0x40000000;
constexpr unsigned SPIN_CAP = 1u << 14;
constexpr float NLOG2E = -1.4426950408889634f;
constexpr float NLN2 = -0.6931471805599453f;
constexpr float INV_N = 1.0f / (32.0f * 128.0f * 128.0f);

typedef __amdgpu_buffer_rsrc_t rsrc_t;

DI float asf(unsigned u) { return __builtin_bit_cast(float, u); }
DI unsigned asu(float f) { return __builtin_bit_cast(unsigned, f); }
DI float bflo(unsigned u) { return __builtin_bit_cast(float, u << 16); }
DI float bfhi(unsigned u) { return __builtin_bit_cast(float, u & 0xffff0000u); }
DI unsigned cvtpk(float lo, float hi) {
  f32x2 v = {lo, hi};
  bf16x2 b = __builtin_convertvector(v, bf16x2);
  return __builtin_bit_cast(unsigned, b);
}
DI unsigned cvtpk_s(float lo, float hi) {
  asm("" : "+v"(lo));
  asm("" : "+v"(hi));
  return cvtpk(lo, hi);
}
DI float dot2(unsigned a, unsigned b, float c) {
  return __builtin_amdgcn_fdot2_f32_bf16(__builtin_bit_cast(bf16x2, a), __builtin_bit_cast(bf16x2, b), c, false);
}
template <int CTRL> DI float dpp_add(float v) {
  int r = __builtin_amdgcn_update_dpp(0, __builtin_bit_cast(int, v), CTRL, 0xf, 0xf, true);
  return v + asf((unsigned)r);
}
DI float swap16_add(float v) {
  unsigned u = asu(v);
  auto r = __builtin_amdgcn_permlane16_swap(u, u, false, false);
  unsigned r0 = r[0], r1 = r[1];
  return asf(r0) + asf(r1);
}
DI float swap32_add(float v) {
  unsigned u = asu(v);
  auto r = __builtin_amdgcn_permlane32_swap(u, u, false, false);
  unsigned r0 = r[0], r1 = r[1];
  return asf(r0) + asf(r1);
}
DI float red_mod4(float v) {
  v = dpp_add<0x124>(v);
  v = dpp_add<0x128>(v);
  v = swap16_add(v);
  return swap32_add(v);
}
DI float red_all(float v) {
  v = dpp_add<0x121>(v);
  v = dpp_add<0x122>(v);
  return red_mod4(v);
}
DI bool all_lanes(bool ok) { return __builtin_amdgcn_ballot_w64(!ok) == 0ull; }
DI void bar() { asm volatile("s_barrier" ::: "memory"); }
DI void bar_lds() { asm volatile("s_waitcnt lgkmcnt(0)\n\ts_barrier" ::: "memory"); }
DI void drain() { asm volatile("s_waitcnt vmcnt(0)" ::: "memory"); }

DI v4u lds128(LAS char* L, int off) { return *(LAS v4u*)(L + off); }
DI void sts128(LAS char* L, int off, v4u v) { *(LAS v4u*)(L + off) = v; }
DI unsigned lds32(LAS char* L, int off) { return *(LAS unsigned*)(L + off); }
DI void sts32(LAS char* L, int off, unsigned v) { *(LAS unsigned*)(L + off) = v; }
DI f32x4 ldsf4(LAS char* L, int off) { return *(LAS f32x4*)(L + off); }
DI f32x2 ldsf2(LAS char* L, int off) { return *(LAS f32x2*)(L + off); }
DI unsigned lds32a(int a) { return *(LAS unsigned*)(size_t)(unsigned)a; }
DI f32x4 ldsf4a(int a) { return *(LAS f32x4*)(size_t)(unsigned)a; }
DI v4u lds128a(int a) { return *(LAS v4u*)(size_t)(unsigned)a; }
DI void sts32a(int a, unsigned v) { *(LAS unsigned*)(size_t)(unsigned)a = v; }
DI v2u ldstr(LAS char* L, int off) {
  typedef short v4i16_t __attribute__((ext_vector_type(4)));
  return __builtin_bit_cast(v2u, __builtin_amdgcn_ds_read_tr16_b64_v4i16((LAS v4i16_t*)(L + off)));
}
DI v4u ld128(rsrc_t r, int v, int s, int) { return __builtin_amdgcn_raw_buffer_load_b128(r, v, s, 0); }
DI v4u ld128c(rsrc_t r, int v, int s) { return __builtin_amdgcn_raw_buffer_load_b128(r, v, s, SC1); }
DI unsigned ld32c(rsrc_t r, int v) { return __builtin_amdgcn_raw_buffer_load_b32(r, v, 0, SC1); }
DI void st128c(v4u d, rsrc_t r, int v) { __builtin_amdgcn_raw_buffer_store_b128(d, r, v, 0, SC1); }

#define ST(i) do {} while (0)

#define SEGT(k) do {} while (0)
DI float cut(float x) { asm("" : "+v"(x)); return x; }
DI float act(float x, float a, float b) {
  float t = cut(__builtin_fmaf(x, a, b));
  float r = __builtin_amdgcn_rcpf(cut(1.0f + __builtin_amdgcn_exp2f(t)));
  return cut(t * r);
}
template <int N>
DI void act_rows(const unsigned* raw, float a0, float b0, float a1, float b1, float (&lo)[N], float (&hi)[N]) {
  float t[2 * N], e[2 * N];
#pragma unroll
  for (int r = 0; r < N; ++r) {
    t[2 * r] = cut(__builtin_fmaf(bflo(raw[r]), a0, b0));
    t[2 * r + 1] = cut(__builtin_fmaf(bfhi(raw[r]), a1, b1));
  }
  __builtin_amdgcn_sched_barrier(0);
#pragma unroll
  for (int i = 0; i < 2 * N; ++i) e[i] = __builtin_amdgcn_exp2f(t[i]);
  __builtin_amdgcn_sched_barrier(0);
#pragma unroll
  for (int i = 0; i < 2 * N; ++i) e[i] = cut(1.0f + e[i]);
  __builtin_amdgcn_sched_barrier(0);
#pragma unroll
  for (int i = 0; i < 2 * N; ++i) e[i] = __builtin_amdgcn_rcpf(e[i]);
  __builtin_amdgcn_sched_barrier(0);
#pragma unroll
  for (int r = 0; r < N; ++r) {
    lo[r] = cut(t[2 * r] * e[2 * r]);
    hi[r] = cut(t[2 * r + 1] * e[2 * r + 1]);
  }
  __builtin_amdgcn_sched_barrier(0);
}
DI void wino_in(const float (&lo)[4], const float (&hi)[4], unsigned (&o)[4]) {
  o[0] = cvtpk_s(lo[0] - lo[2], hi[0] - hi[2]);
  o[1] = cvtpk_s(lo[1] + lo[2], hi[1] + hi[2]);
  o[2] = cvtpk_s(lo[2] - lo[1], hi[2] - hi[1]);
  o[3] = cvtpk_s(lo[1] - lo[3], hi[1] - hi[3]);
}
DI void group_stats(LAS char* L, int off, float& mean, float& rstd) {
  f32x4 p0 = ldsf4(L, off), p1 = ldsf4(L, off + 16);
  float s0 = cut(p0[0]), q0 = cut(p0[1]), s1 = cut(p0[2]), q1 = cut(p0[3]);
  float s2 = cut(p1[0]), q2 = cut(p1[1]), s3 = cut(p1[2]), q3 = cut(p1[3]);
  float S = cut(cut(s0 + s1) + cut(s2 + s3));
  float Q = cut(cut(q0 + q1) + cut(q2 + q3));
  mean = cut(S * INV_N);
  float var = __builtin_fmaxf(cut(Q * INV_N) - cut(mean * mean), 0.f);
  rstd = __builtin_amdgcn_rsqf(var + 1e-5f);
}

struct Ctx {
  LAS char* L;
  char* ws;
  rsrc_t rsX, rsO, rsW;
  int tid, lane, w, wg, b, ty, tx, loc;
};

DI void transpose_rows(const Ctx& c, int ch0, int row) {
  LAS char* L = c.L;
  const int lane = c.lane, h = lane >> 5, j = lane & 31;
  const int src = L_STG + row * 8192 + (ch0 + 8 * h + ((lane & 15) >> 2)) * 64 + (((lane >> 4) & 1) * 16 + (lane & 3) * 4) * 2;
  v2u lo = ldstr(L, src), hi = ldstr(L, src + 256);
  sts128(L, L_RAW + ((ch0 >> 3) + h) * PLR + (j + 1) * CSR + row * 16, v4u{lo[0], lo[1], hi[0], hi[1]});
}

DI void gather(const Ctx& c, int off, unsigned tag, int partoff) {
  v4u a, d;
  for (unsigned spins = 0;; ++spins) {
    asm volatile("" ::: "memory");
    a = ld128c(c.rsW, off, 0);
    d = ld128c(c.rsW, off + 1024, 0);
    bool ok = (a[1] == tag) & (a[3] == tag) & (d[1] == tag) & (d[3] == tag);
    if (all_lanes(ok) || spins > SPIN_CAP) break;
  }
  unsigned a0 = a[0], a2 = a[2], d0 = d[0], d2 = d[2];
  float s = red_mod4(asf(a0) + asf(d0));
  float q = red_mod4(asf(a2) + asf(d2));
  if (c.lane < 4) *(LAS f32x2*)(c.L + partoff + c.lane * 32 + c.w * 8) = f32x2{s, q};
}

template <int CONV>
DI void halo_cols(const Ctx& c, const unsigned (&raw)[4], bool valid, bool top, bool bot) {
  LAS char* L = c.L;
  const int lane = c.lane, pair = lane >> 5, oc = 4 * c.w + ((lane >> 3) & 3), side = (lane >> 2) & 1, pr = lane & 3;
  float mean, rstd;
  group_stats(L, L_PART + CONV * 128 + c.w * 32, mean, rstd);
  const int ch = 8 * oc + 2 * pr;
  f32x2 g = ldsf2(L, L_GB + CONV * 1024 + ch * 4), be = ldsf2(L, L_GB + CONV * 1024 + 512 + ch * 4);
  float a0 = g[0] * rstd, a1 = g[1] * rstd;
  float b0 = be[0] - a0 * mean, b1 = be[1] - a1 * mean;
  float lo[4], hi[4];
  act_rows<4>(raw, a0, b0, a1, b1, lo, hi);
#pragma unroll
  for (int r = 0; r < 4; ++r) {
    bool v = valid && !(r == 0 && pair == 0 && top) && !(r == 3 && pair == 1 && bot);
    lo[r] = v ? lo[r] : 0.f;
    hi[r] = v ? hi[r] : 0.f;
  }
  unsigned o[4];
  wino_in(lo, hi, o);
  const int dst = L_V + oc * PLV + (side ? 33 : 0) * CSV + pair * 16 + pr * 4;
#pragma unroll
  for (int xi = 0; xi < 4; ++xi) sts32(L, dst + xi * 32, o[xi]);
}

template <int CONV>
DI void mainloop(const Ctx& c, f32x16 (&acc)[8], v4u (&wr)[12], int wbase, int wnext) {
  LAS char* L = c.L;
  const int lane = c.lane, j = lane & 31, h = lane >> 5;
  constexpr int QS = CONV == 0 ? 16 : CSV;
  int ab = (int)(size_t)L + L_V + h * PLV + (CONV == 0 ? j * CSV : (j >> 4) * 16 + (j & 15) * 2 * CSV);
  asm volatile("" : "+v"(ab));
  const int wv = lane * 16;
  v4u bf[4][2];
  bar_lds();
  {
    char* ws = c.ws; const int w = c.w, wg = c.wg;
    ST(8 + CONV);
  }
#pragma unroll
  for (int s = 0; s < 3; ++s)
#pragma unroll
    for (int q = 0; q < 2; ++q) bf[s][q] = lds128a(ab + q * QS + (s & 3) * 32 + (s >> 2) * CSV);
#pragma unroll 1
  for (int ck = 0; ck < 8; ck += 2) {
    asm volatile("" : "+v"(ab));
    const int wn0 = wbase + (ck + 1) * 12288;
    const int wn1 = (ck < 6) ? wbase + (ck + 2) * 12288 : wnext;
#pragma unroll
    for (int s2 = 0; s2 < 24; ++s2) {
      const int s = s2 % 12;
      if (s == 9) bar();
      const int sn = (s + 3) % 12;
      const int cn = ((s2 + 3) / 12) * 2 * PLV;
#pragma unroll
      for (int q = 0; q < 2; ++q) {
        bf[(s + 3) & 3][q] = lds128a(ab + cn + q * QS + (sn & 3) * 32 + (sn >> 2) * CSV);
        acc[(s & 3) * 2 + q] = __builtin_amdgcn_mfma_f32_32x32x16_bf16(
            __builtin_bit_cast(bf16x8, wr[s]), __builtin_bit_cast(bf16x8, bf[s & 3][q]), acc[(s & 3) * 2 + q], 0, 0, 0);
        __builtin_amdgcn_sched_barrier(0);
      }
      wr[s] = ld128c(c.rsW, wv, (s2 < 12 ? wn0 : wn1) + s * 1024);
      __builtin_amdgcn_sched_barrier(0);
    }
    ab += 4 * PLV;
  }
}

DI void prod_affine(const unsigned (&raw)[6], f32x4 ab, float (&t)[12]) {
  asm("v_lshlrev_b32 %0, 16, %12\n\tv_and_b32 %1, 0xffff0000, %12\n\t"
      "v_lshlrev_b32 %2, 16, %13\n\tv_and_b32 %3, 0xffff0000, %13\n\t"
      "v_lshlrev_b32 %4, 16, %14\n\tv_and_b32 %5, 0xffff0000, %14\n\t"
      "v_lshlrev_b32 %6, 16, %15\n\tv_and_b32 %7, 0xffff0000, %15\n\t"
      "v_lshlrev_b32 %8, 16, %16\n\tv_and_b32 %9, 0xffff0000, %16\n\t"
      "v_lshlrev_b32 %10, 16, %17\n\tv_and_b32 %11, 0xffff0000, %17\n\t"
      "v_fma_f32 %0, %0, %18, %20\n\tv_fma_f32 %1, %1, %19, %21\n\t"
      "v_fma_f32 %2, %2, %18, %20\n\tv_fma_f32 %3, %3, %19, %21\n\t"
      "v_fma_f32 %4, %4, %18, %20\n\tv_fma_f32 %5, %5, %19, %21\n\t"
      "v_fma_f32 %6, %6, %18, %20\n\tv_fma_f32 %7, %7, %19, %21\n\t"
      "v_fma_f32 %8, %8, %18, %20\n\tv_fma_f32 %9, %9, %19, %21\n\t"
      "v_fma_f32 %10, %10, %18, %20\n\tv_fma_f32 %11, %11, %19, %21"
      : "=&v"(t[0]), "=&v"(t[1]), "=&v"(t[2]), "=&v"(t[3]), "=&v"(t[4]), "=&v"(t[5]), "=&v"(t[6]), "=&v"(t[7]),
        "=&v"(t[8]), "=&v"(t[9]), "=&v"(t[10]), "=&v"(t[11])
      : "v"(raw[0]), "v"(raw[1]), "v"(raw[2]), "v"(raw[3]), "v"(raw[4]), "v"(raw[5]), "v"(ab[0]), "v"(ab[1]),
        "v"(ab[2]), "v"(ab[3]));
}
DI void prod_sigmoid(const float (&t)[12], float (&e)[12]) {
  asm("v_exp_f32 %0, %12\n\tv_exp_f32 %1, %13\n\tv_exp_f32 %2, %14\n\tv_exp_f32 %3, %15\n\t"
      "v_exp_f32 %4, %16\n\tv_exp_f32 %5, %17\n\tv_exp_f32 %6, %18\n\tv_exp_f32 %7, %19\n\t"
      "v_exp_f32 %8, %20\n\tv_exp_f32 %9, %21\n\tv_exp_f32 %10, %22\n\tv_exp_f32 %11, %23\n\t"
      "v_add_f32 %0, 1.0, %0\n\tv_add_f32 %1, 1.0, %1\n\tv_add_f32 %2, 1.0, %2\n\tv_add_f32 %3, 1.0, %3\n\t"
      "v_add_f32 %4, 1.0, %4\n\tv_add_f32 %5, 1.0, %5\n\tv_add_f32 %6, 1.0, %6\n\tv_add_f32 %7, 1.0, %7\n\t"
      "v_add_f32 %8, 1.0, %8\n\tv_add_f32 %9, 1.0, %9\n\tv_add_f32 %10, 1.0, %10\n\tv_add_f32 %11, 1.0, %11\n\t"
      "v_rcp_f32 %0, %0\n\tv_rcp_f32 %1, %1\n\tv_rcp_f32 %2, %2\n\tv_rcp_f32 %3, %3\n\t"
      "v_rcp_f32 %4, %4\n\tv_rcp_f32 %5, %5\n\tv_rcp_f32 %6, %6\n\tv_rcp_f32 %7, %7\n\t"
      "v_rcp_f32 %8, %8\n\tv_rcp_f32 %9, %9\n\tv_rcp_f32 %10, %10\n\tv_rcp_f32 %11, %11\n\ts_nop 0"
      : "=&v"(e[0]), "=&v"(e[1]), "=&v"(e[2]), "=&v"(e[3]), "=&v"(e[4]), "=&v"(e[5]), "=&v"(e[6]), "=&v"(e[7]),
        "=&v"(e[8]), "=&v"(e[9]), "=&v"(e[10]), "=&v"(e[11])
      : "v"(t[0]), "v"(t[1]), "v"(t[2]), "v"(t[3]), "v"(t[4]), "v"(t[5]), "v"(t[6]), "v"(t[7]), "v"(t[8]),
        "v"(t[9]), "v"(t[10]), "v"(t[11]));
}
DI void prod_wino(float (&t)[12], float (&e)[12], unsigned (&v)[8]) {
  asm("v_mul_f32 %2, %2, %14\n\tv_mul_f32 %3, %3, %15\n\t"
      "v_mul_f32 %4, %4, %16\n\tv_mul_f32 %5, %5, %17\n\t"
      "v_mul_f32 %6, %6, %18\n\tv_mul_f32 %7, %7, %19\n\t"
      "v_mul_f32 %8, %8, %20\n\tv_mul_f32 %9, %9, %21\n\t"
      "v_fma_f32 %12, %0, %12, -%4\n\tv_fma_f32 %13, %1, %13, -%5\n\t"
      "v_fma_f32 %22, -%10, %22, %6\n\tv_fma_f32 %23, -%11, %23, %7\n\t"
      "v_add_f32 %14, %2, %4\n\tv_add_f32 %15, %3, %5\n\t"
      "v_sub_f32 %16, %4, %2\n\tv_sub_f32 %17, %5, %3\n\t"
      "v_sub_f32 %18, %2, %6\n\tv_sub_f32 %19, %3, %7\n\t"
      "v_sub_f32 %20, %4, %8\n\tv_sub_f32 %21, %5, %9\n\t"
      "v_add_f32 %0, %6, %8\n\tv_add_f32 %1, %7, %9\n\t"
      "v_sub_f32 %10, %8, %6\n\tv_sub_f32 %11, %9, %7\n\t"
      "v_cvt_pk_bf16_f32 %12, %12, %13\n\tv_cvt_pk_bf16_f32 %22, %22, %23\n\t"
      "v_cvt_pk_bf16_f32 %14, %14, %15\n\tv_cvt_pk_bf16_f32 %16, %16, %17\n\t"
      "v_cvt_pk_bf16_f32 %18, %18, %19\n\tv_cvt_pk_bf16_f32 %20, %20, %21\n\t"
      "v_cvt_pk_bf16_f32 %0, %0, %1\n\tv_cvt_pk_bf16_f32 %10, %10, %11"
      : "+v"(t[0]), "+v"(t[1]), "+v"(t[2]), "+v"(t[3]), "+v"(t[4]), "+v"(t[5]), "+v"(t[6]), "+v"(t[7]), "+v"(t[8]),
        "+v"(t[9]), "+v"(t[10]), "+v"(t[11]), "+v"(e[0]), "+v"(e[1]), "+v"(e[2]), "+v"(e[3]), "+v"(e[4]), "+v"(e[5]),
        "+v"(e[6]), "+v"(e[7]), "+v"(e[8]), "+v"(e[9]), "+v"(e[10]), "+v"(e[11]));
  v[0] = asu(e[0]); v[1] = asu(e[8]); v[2] = asu(e[2]); v[3] = asu(t[0]);
  v[4] = asu(e[4]); v[5] = asu(t[10]); v[6] = asu(e[6]); v[7] = asu(e[10]);
}

template <int CONV>
DI void make_ab(const Ctx& c, int p) {
  LAS char* L = c.L;
  float mean, rstd;
  group_stats(L, L_PART + CONV * 128 + (c.lane >> 4) * 32, mean, rstd);
  f32x2 g = ldsf2(L, L_GB + CONV * 1024 + c.lane * 8), be = ldsf2(L, L_GB + CONV * 1024 + 512 + c.lane * 8);
  float a0 = g[0] * rstd, a1 = g[1] * rstd;
  *(LAS f32x4*)(L + L_AB + p * 1024 + c.lane * 16) = f32x4{a0, a1, be[0] - a0 * mean, be[1] - a1 * mean};
}

template <int CONV>
DI void produce(const Ctx& c, bool top, bool bot, unsigned tag) {
  LAS char* L = c.L;
  const int lane = c.lane, p = c.w - 4, w = c.w, wg = c.wg;
  char* ws = c.ws;
  const int o = lane >> 5, col = 8 * p + ((lane >> 2) & 7), pr = lane & 3;
  const int lb = (int)(size_t)L;
  int src = lb + L_RAW + o * PLR + (col + 1) * CSR + pr * 4;
  int dst = lb + L_V + o * PLV + (col + 1) * CSV + pr * 4;
  int abo = lb + L_AB + p * 1024 + (4 * o + pr) * 16;
  asm volatile("" : "+v"(src), "+v"(dst), "+v"(abo));
  unsigned raw[6];
#pragma unroll
  for (int r = 0; r < 6; ++r) raw[r] = lds32a(src + r * 16);
  f32x4 ab = ldsf4a(abo);
  int edge = top ? 1 : bot ? 2 : 0;
  asm volatile("" : "+s"(edge));
  const float ktop = top ? 0.f : 1.f, kbot = bot ? 0.f : 1.f;
#pragma unroll 1
  for (int ck = 0; ck < 8; ++ck) {
    float t[12], e[12];
    prod_affine(raw, ab, t);
    __builtin_amdgcn_sched_barrier(0);
    SEGT(0);
    if (ck > 0) bar_lds();
    SEGT(1);
    src += 2 * PLR;
    abo += 128;
    asm volatile("" : "+v"(src), "+v"(abo));
#pragma unroll
    for (int r = 0; r < 6; ++r) raw[r] = lds32a(src + r * 16);
    ab = ldsf4a(abo);
    __builtin_amdgcn_sched_barrier(0);
    prod_sigmoid(t, e);
    if (edge) {
      asm volatile("v_mul_f32 %0, %4, %0\n\tv_mul_f32 %1, %4, %1\n\tv_mul_f32 %2, %5, %2\n\tv_mul_f32 %3, %5, %3"
                   : "+v"(e[0]), "+v"(e[1]), "+v"(e[10]), "+v"(e[11]) : "s"(ktop), "s"(kbot));
    }
    unsigned v[8];
    prod_wino(t, e, v);
#pragma unroll
    for (int i = 0; i < 8; ++i) sts32a(dst + i * 16, v[i]);
    dst += 2 * PLV;
    asm volatile("" : "+v"(dst));
    __builtin_amdgcn_sched_barrier(0);
    SEGT(2);
    if (CONV == 0 && ck == 4 && p == 3) {
      float e0 = 0.f, e1 = 0.f;
      for (unsigned spins = 0;; ++spins) {
        asm volatile("" ::: "memory");
        bool ok = true;
        e0 = 0.f; e1 = 0.f;
#pragma unroll
        for (int kr = 0; kr < 8; ++kr) {
          v4u g = ld128c(c.rsW, WS_EMB + (c.b * 128 + kr * 16 + (lane >> 2)) * 64 + (lane & 3) * 16, 0);
          unsigned g0 = g[0], g2 = g[2];
          ok = ok & (g[1] == tag) & (g[3] == tag);
          e0 += asf(g0);
          e1 += asf(g2);
        }
        if (all_lanes(ok) || spins > SPIN_CAP) break;
      }
      *(LAS f32x2*)(L + L_EMB + lane * 8) = f32x2{e0, e1};
    }
  }
  bar_lds();
  bar();
}

DI void publish(const Ctx& c, int rec, float s, float q, unsigned tag) {
  st128c(v4u{asu(s), tag, asu(q), tag}, c.rsW, rec + (c.b * 128 + c.loc) * 64 + c.lane * 16);
}

DI void consumer(const Ctx& c, const u16* wsrc) {
  LAS char* L = c.L;
  char* ws = c.ws;
  const int lane = c.lane, w = c.w, wg = c.wg, b = c.b, ty = c.ty, tx = c.tx, loc = c.loc;
  const unsigned xold = __builtin_amdgcn_raw_buffer_load_b32(c.rsW, b * 256, 0, 0);
  v4u xi[8];
  const int xoff = ((b * 128 + 32 * w + (lane >> 2)) * 128 + ty * 4) * 256 + tx * 64 + (lane & 3) * 16;
  if (w < 2) {
    const int wn = (loc >> 4) & 3, wc = (loc >> 1) & 7, woh = loc & 1;
    const int oo = lane >> 2, pr = lane & 3;
    const char* src = (const char*)wsrc + ((32 * wn + 16 * woh + oo) * 128 + 16 * wc + 8 * w + 2 * pr) * 18;
    v3u d0 = *(const v3u*)(src), d1 = *(const v3u*)(src + 12), d2 = *(const v3u*)(src + 24);
#pragma unroll
    for (int i = 0; i < 8; ++i) xi[i] = ld128(c.rsX, xoff + (i & 3) * 256, (i >> 2) * 16 * 32768, 0);
    bar();
    ST(14);
    const unsigned d[9] = {d0[0], d0[1], d0[2], d1[0], d1[1], d1[2], d2[0], d2[1], d2[2]};
    float e[18];
#pragma unroll
    for (int i = 0; i < 9; ++i) { e[2 * i] = bflo(d[i]); e[2 * i + 1] = bfhi(d[i]); }
    const int sc = L_WT + w * 12 * 256 + lane * 4;
#pragma unroll
    for (int dx = 0; dx < 3; ++dx) {
      float a0 = e[dx], a1 = e[3 + dx], a2 = e[6 + dx], b0 = e[9 + dx], b1 = e[12 + dx], b2 = e[15 + dx];
      float ta = a0 + a2, tb = b0 + b2;
      sts32(L, sc + (dx * 4 + 0) * 256, cvtpk(a0 * NLN2, b0 * NLN2));
      sts32(L, sc + (dx * 4 + 1) * 256, cvtpk((ta + a1) * (0.5f * NLN2), (tb + b1) * (0.5f * NLN2)));
      sts32(L, sc + (dx * 4 + 2) * 256, cvtpk((ta - a1) * (0.5f * NLN2), (tb - b1) * (0.5f * NLN2)));
      sts32(L, sc + (dx * 4 + 3) * 256, cvtpk(a2 * NLN2, b2 * NLN2));
    }
    const int dst = WS_WT + b * WT_COPY + (loc >> 6) * WT_CONV + wn * WT_WAVE + wc * 12288 + (w * 32 + woh * 16 + oo) * 16;
#pragma unroll
    for (int g = 0; g < 3; ++g) {
      v4u o = lds128(L, L_WT + w * 12 * 256 + (4 * g + pr) * 256 + oo * 16);
      st128c(o, c.rsW, dst + (4 * g + pr) * 1024);
    }
  } else {
#pragma unroll
    for (int i = 0; i < 8; ++i) xi[i] = ld128(c.rsX, xoff + (i & 3) * 256, (i >> 2) * 16 * 32768, 0);
    bar();
  }
  ST(12);
  {
    float s = 0.f, q = 0.f;
#pragma unroll
    for (int i = 0; i < 8; ++i)
#pragma unroll
      for (int d = 0; d < 4; ++d) {
        unsigned u = xi[i][d];
        s = dot2(u, 0x3f803f80u, s);
        q = dot2(u, u, q);
      }
    s = red_all(s);
    q = red_all(q);
    ST(13);
    const unsigned tag0 = __builtin_amdgcn_readfirstlane(xold) * 0x9E3779B1u + 0x7F4A7C15u;
    if (w < 2) drain();
    if (lane == 0) sts128(L, L_RED + w * 16, v4u{asu(s), asu(q), tag0, 0u});
  }
  const unsigned tag = __builtin_amdgcn_readfirstlane(xold) * 0x9E3779B1u + 0x7F4A7C15u;
  if (w == 0) {
    v4u v;
    for (unsigned spins = 0;; ++spins) {
      asm volatile("" ::: "memory");
      v = lds128(L, L_RED + (lane & 3) * 16);
      if (all_lanes(v[2] == tag) || spins > (SPIN_CAP << 4)) break;
    }
    unsigned v0 = v[0], v1 = v[1];
    if (lane < 4) publish(c, WS_REC1, asf(v0), asf(v1), tag);
  }
  ST(1);
#pragma unroll
  for (int i = 0; i < 8; ++i)
    sts128(L, L_STG + ((i & 3) + 1) * 8192 + (32 * w + 16 * (i >> 2)) * 64 + lane * 16, xi[i]);
#pragma unroll
  for (int i = 0; i < 8; ++i) transpose_rows(c, 32 * w + 16 * (i >> 2), (i & 3) + 1);
  __builtin_amdgcn_s_setprio(PRIO_C);
  gather(c, WS_REC1 + b * 8192 + w * 2048 + lane * 16, tag, L_PART);
  bar_lds();
  ST(2);
  const int wb1 = WS_WT + b * WT_COPY + w * WT_WAVE, wb2 = wb1 + WT_CONV;
  v4u wr[12];
#pragma unroll
  for (int s = 0; s < 12; ++s) wr[s] = ld128c(c.rsW, lane * 16, wb1 + s * 1024);
  const int hpair = lane >> 5, hoc = 4 * w + ((lane >> 3) & 3), hside = (lane >> 2) & 1, hpr = lane & 3;
  const bool top = ty == 0, bot = ty == 31;
  const bool cvalid = hside ? tx < 3 : tx > 0;
  {
    unsigned raw[4];
#pragma unroll
    for (int r = 0; r < 4; ++r)
      raw[r] = lds32(L, L_RAW + hoc * PLR + (hside ? 33 : 0) * CSR + (2 * hpair + r) * 16 + hpr * 4);
    halo_cols<0>(c, raw, cvalid, top, bot);
  }
  f32x16 acc[8];
#pragma unroll
  for (int m = 0; m < 8; ++m)
#pragma unroll
    for (int r = 0; r < 16; ++r) acc[m][r] = 0.f;
  mainloop<0>(c, acc, wr, wb1, wb2);
  ST(3);
  const int j = lane & 31, h = lane >> 5;
  const int ooff = ((b * 128 + 32 * w + 4 * h) * 128 + ty * 4 + 2 * (j >> 4)) * 256 + tx * 64 + (j & 15) * 4;
  {
    f32x4 ev[4];
#pragma unroll
    for (int k = 0; k < 4; ++k) ev[k] = ldsf4(L, L_EMB + (32 * w + 8 * k + 4 * h) * 4);
    float s = 0.f, q = 0.f;
    const int hb = WS_HALO + (b * 128 + loc) * HALO_WG;
    const int ldst = L_RAW + (4 * w + h) * PLR + (j + 1) * CSR + 16;
#pragma unroll
    for (int mi = 0; mi < 4; ++mi) {
      const int m = mi == 0 ? 0 : mi == 1 ? 3 : mi - 1;
      const int pq = m >> 1;
      unsigned pk[4][2];
#pragma unroll
      for (int k = 0; k < 4; ++k) {
        f32x2 v[2];
#pragma unroll
        for (int i = 0; i < 2; ++i) {
          const int r = 4 * k + 2 * i;
          f32x2 m0 = {acc[0 + pq][r], acc[0 + pq][r + 1]}, m1 = {acc[2 + pq][r], acc[2 + pq][r + 1]};
          f32x2 m2 = {acc[4 + pq][r], acc[4 + pq][r + 1]}, m3 = {acc[6 + pq][r], acc[6 + pq][r + 1]};
          f32x2 ee = {ev[k][2 * i], ev[k][2 * i + 1]};
          v[i] = (m & 1) ? ((m1 + ee) - m2) - m3 : ((m1 + ee) + m2) + m0;
        }
        pk[k][0] = cvtpk(v[0][0], v[0][1]);
        pk[k][1] = cvtpk(v[1][0], v[1][1]);
        s = dot2(pk[k][0], 0x3f803f80u, s);
        q = dot2(pk[k][0], pk[k][0], q);
        s = dot2(pk[k][1], 0x3f803f80u, s);
        q = dot2(pk[k][1], pk[k][1], q);
      }
#pragma unroll
      for (int t = 0; t < 2; ++t) {
        auto r0 = __builtin_amdgcn_permlane32_swap(pk[2 * t][0], pk[2 * t + 1][0], false, false);
        auto r1 = __builtin_amdgcn_permlane32_swap(pk[2 * t][1], pk[2 * t + 1][1], false, false);
        v4u o = {r0[0], r1[0], r0[1], r1[1]};
        if (m == 0 || m == 3) {
          st128c(o, c.rsW, hb + (m == 0 ? 0 : 8192) + (w * 2 + t) * 1024 + lane * 16);
        } else {
          int off = hb + 16384 + (w * 2 + t) * 1024 + ((j == 31 ? 4 : 0) + (m - 1) * 2 + h) * 16;
          if (j != 0 && j != 31) off = OOB;
          st128c(o, c.rsW, off);
        }
        sts128(L, ldst + 2 * t * PLR + m * 16, o);
      }
    }
    s = red_all(s);
    q = red_all(q);
    if (lane == 0) *(LAS f32x2*)(L + L_RED + 96 + w * 8) = f32x2{s, q};
  }
  drain();
  bar_lds();
  ST(4);
  if (w == 0 && lane < 4) {
    f32x2 v = ldsf2(L, L_RED + 96 + lane * 8);
    publish(c, WS_REC2, v[0], v[1], tag);
  }
  gather(c, WS_REC2 + b * 8192 + w * 2048 + lane * 16, tag, L_PART + 128);
  bar_lds();
  {
    const int xo = L_XK + 2 * (j >> 4) * XKR + (32 * w + 4 * h) * 64 + (j & 15) * 4;
#pragma unroll
    for (int r = 0; r < 16; ++r) {
      const unsigned u0 = lds32(L, xo + ((r & 3) + 8 * (r >> 2)) * 64);
      const unsigned u1 = lds32(L, xo + XKR + ((r & 3) + 8 * (r >> 2)) * 64);
      acc[0][r] = bflo(u0);
      acc[1][r] = bfhi(u0);
      acc[6][r] = -bflo(u1);
      acc[7][r] = -bfhi(u1);
      acc[2][r] = 0.f; acc[3][r] = 0.f; acc[4][r] = 0.f; acc[5][r] = 0.f;
    }
  }
  asm volatile("s_waitcnt lgkmcnt(0)" ::: "memory");
  bar();
  ST(5);
  {
    unsigned raw[4];
#pragma unroll
    for (int r = 0; r < 4; ++r)
      raw[r] = lds32(L, L_RAW + hoc * PLR + (hside ? 33 : 0) * CSR + (2 * hpair + r) * 16 + hpr * 4);
    halo_cols<1>(c, raw, cvalid, top, bot);
  }
  mainloop<1>(c, acc, wr, wb2, wb2);
  ST(6);
#pragma unroll
  for (int r = 0; r < 16; r += 2) {
    f32x2 y0[2], y1[2];
#pragma unroll
    for (int q = 0; q < 2; ++q) {
      f32x2 m0 = {acc[0 + q][r], acc[0 + q][r + 1]}, m1 = {acc[2 + q][r], acc[2 + q][r + 1]};
      f32x2 m2 = {acc[4 + q][r], acc[4 + q][r + 1]}, m3 = {acc[6 + q][r], acc[6 + q][r + 1]};
      y0[q] = (m1 + m2) + m0;
      y1[q] = (m1 - m2) - m3;
    }
#pragma unroll
    for (int i = 0; i < 2; ++i) {
      const int so = (((r + i) & 3) + 8 * ((r + i) >> 2)) * 32768;
      __builtin_amdgcn_raw_buffer_store_b32(cvtpk(y0[0][i], y0[1][i]), c.rsO, ooff, so, 0);
      __builtin_amdgcn_raw_buffer_store_b32(cvtpk(y1[0][i], y1[1][i]), c.rsO, ooff + 256, so, 0);
    }
  }
  ST(7);
}

DI void producer(const Ctx& c, const u16* gtab, const u16* temb, const u16* wemb) {
  LAS char* L = c.L;
  char* ws = c.ws;
  const int lane = c.lane, w = c.w, wg = c.wg, b = c.b, ty = c.ty, tx = c.tx, loc = c.loc, p = w - 4;
  const unsigned xold = __builtin_amdgcn_raw_buffer_load_b32(c.rsW, b * 256, 0, 0);
  bar();
  const unsigned tb0 = gtab[lane], tb1 = gtab[lane + 64];
  unsigned te0 = 0, te1 = 0;
  v4u we0 = {0u, 0u, 0u, 0u}, we1 = {0u, 0u, 0u, 0u};
  if (p == 3) {
    const int k0 = 96 * (loc >> 4) + lane, k1 = k0 + 64;
    te0 = temb[b * 768 + k0];
    we0 = *(const v4u*)(wemb + k0 * 128 + (loc & 15) * 8);
    if (lane < 32) {
      te1 = temb[b * 768 + k1];
      we1 = *(const v4u*)(wemb + k1 * 128 + (loc & 15) * 8);
    }
  }
  const unsigned tag = __builtin_amdgcn_readfirstlane(xold) * 0x9E3779B1u + 0x7F4A7C15u;
  for (unsigned spins = 0;; ++spins) {
    asm volatile("" ::: "memory");
    unsigned t = lds32(L, L_RED + (lane & 3) * 16 + 8);
    if (all_lanes(t == tag) || spins > (SPIN_CAP << 4)) break;
    __builtin_amdgcn_s_sleep(1);
  }
  ST(12);
  v4u xh[4];
  unsigned xc[6];
#pragma unroll
  for (int i = 0; i < 4; ++i) {
    int y = (i & 1) ? ty * 4 + 4 : ty * 4 - 1;
    int off = ((b * 128 + 32 * p + 16 * (i >> 1) + (lane >> 2)) * 128 + y) * 256 + tx * 64 + (lane & 3) * 16;
    if ((unsigned)y > 127u) off = OOB;
    xh[i] = ld128(c.rsX, off, 0, 0);
  }
#pragma unroll
  for (int i = 0; i < 6; ++i) {
    int y = ty * 4 - 1 + i, xx = (lane >> 5) ? tx * 32 + 32 : tx * 32 - 1;
    int off = ((b * 128 + 32 * p + (lane & 31)) * 128 + y) * 256 + xx * 2;
    if ((unsigned)y > 127u || (unsigned)xx > 127u) off = OOB;
    xc[i] = __builtin_amdgcn_raw_buffer_load_b16(c.rsX, off, 0, 0);
  }
#pragma unroll
  for (int i = 0; i < 4; ++i)
    sts128(L, L_STG + ((i & 1) ? 5 : 0) * 8192 + (32 * p + 16 * (i >> 1)) * 64 + lane * 16, xh[i]);
  ST(1);
#pragma unroll
  for (int i = 0; i < 4; ++i) transpose_rows(c, 32 * p + 16 * (i >> 1), (i & 1) ? 5 : 0);
#pragma unroll
  for (int i = 0; i < 6; ++i) {
    const int ch = 32 * p + (lane & 31);
    *(LAS u16*)(L + L_RAW + (ch >> 3) * PLR + ((lane >> 5) ? 33 : 0) * CSR + i * 16 + (ch & 7) * 2) = (u16)xc[i];
  }
  *(LAS float*)(L + L_GB + p * 512 + lane * 4) = asf(tb0 << 16) * NLOG2E;
  *(LAS float*)(L + L_GB + p * 512 + 256 + lane * 4) = asf(tb1 << 16) * NLOG2E;
  if (p == 3) {
    float t0 = asf(te0 << 16), t1 = asf(te1 << 16);
    float s0 = t0 * __builtin_amdgcn_rcpf(1.0f + __builtin_amdgcn_exp2f(t0 * NLOG2E));
    float s1 = t1 * __builtin_amdgcn_rcpf(1.0f + __builtin_amdgcn_exp2f(t1 * NLOG2E));
    s0 = bflo(cvtpk(s0, s0));
    s1 = bflo(cvtpk(s1, s1));
    float pe[8];
#pragma unroll
    for (int d = 0; d < 4; ++d) {
      unsigned u0 = we0[d], u1 = we1[d];
      pe[2 * d] = red_all(s0 * bflo(u0) + s1 * bflo(u1));
      pe[2 * d + 1] = red_all(s0 * bfhi(u0) + s1 * bfhi(u1));
    }
    const int q = lane & 3;
    float va = q == 0 ? pe[0] : q == 1 ? pe[2] : q == 2 ? pe[4] : pe[6];
    float vb = q == 0 ? pe[1] : q == 1 ? pe[3] : q == 2 ? pe[5] : pe[7];
    if (lane < 4) publish(c, WS_EMB, va, vb, tag);
  }
  __builtin_amdgcn_s_setprio(PRIO_P);
  const bool top = ty == 0, bot = ty == 31;
  bar_lds();
  ST(20);
  if (p == 3 && lane == 0 && loc == 0) __builtin_amdgcn_raw_buffer_store_b32(tag, c.rsW, b * 256, 0, SC1);
  make_ab<0>(c, p);
  produce<0>(c, top, bot, tag);
  v4u keep[8];
#pragma unroll
  for (int i = 0; i < 8; ++i)
    keep[i] = ld128(c.rsX, ((b * 128 + 32 * p + (lane >> 2)) * 128 + ty * 4 + (i & 3)) * 256 + tx * 64 + (lane & 3) * 16,
                    (i >> 2) * 16 * 32768, 0);
  bar();
#pragma unroll
  for (int i = 0; i < 8; ++i) sts128(L, L_XK + (i & 3) * XKR + (32 * p + 16 * (i >> 2)) * 64 + lane * 16, keep[i]);
  {
    const int dy = lane < 3 ? -1 : lane < 5 ? 0 : 1;
    const int dxx = lane == 0 || lane == 3 || lane == 5 ? -1 : lane == 1 || lane == 6 ? 0 : 1;
    const int ny = ty + dy, nx = tx + dxx;
    int off = WS_REC2 + (b * 128 + ny * 4 + nx) * 64 + 4;
    const bool need = lane < 8 && (unsigned)ny < 32u && (unsigned)nx < 4u;
    if (!need) off = OOB;
    for (unsigned spins = 0;; ++spins) {
      asm volatile("" ::: "memory");
      unsigned t = ld32c(c.rsW, off);
      if (all_lanes(!need || t == tag) || spins > SPIN_CAP) break;
    }
  }
  v4u hr[4], hcv;
  int hcd;
  {
    const int up = WS_HALO + (b * 128 + loc - 4) * HALO_WG + 8192, dn = WS_HALO + (b * 128 + loc + 4) * HALO_WG;
#pragma unroll
    for (int i = 0; i < 4; ++i) {
      int off = ((i & 1) ? dn : up) + (2 * p + (i >> 1)) * 1024 + lane * 16;
      if ((i & 1) ? bot : top) off = OOB;
      hr[i] = ld128c(c.rsW, off, 0);
    }
    const int q = lane % 12, oc = 4 * p + lane / 12, rr = q >> 1, sd = q & 1;
    const int nl = loc + (sd ? 1 : -1) + (rr == 0 ? -4 : rr == 5 ? 4 : 0);
    const int base = WS_HALO + (b * 128 + nl) * HALO_WG + (oc >> 1) * 1024;
    const int col = sd ? 0 : 31, hh = oc & 1;
    int off;
    if (rr == 0 || rr == 4) off = base + 8192 + (hh * 32 + col) * 16;
    else if (rr == 5 || rr == 1) off = base + (hh * 32 + col) * 16;
    else off = base + 16384 + ((sd ? 0 : 4) + (rr - 2) * 2 + hh) * 16;
    if (lane >= 48 || (sd ? tx == 3 : tx == 0) || (rr == 0 && top) || (rr == 5 && bot)) off = OOB;
    hcv = ld128c(c.rsW, off, 0);
    hcd = L_RAW + oc * PLR + (sd ? 33 : 0) * CSR + rr * 16;
  }
  bar_lds();
  ST(24);
  make_ab<1>(c, p);
#pragma unroll
  for (int i = 0; i < 4; ++i)
    sts128(L, L_RAW + (2 * (2 * p + (i >> 1)) + (lane >> 5)) * PLR + ((lane & 31) + 1) * CSR + ((i & 1) ? 5 : 0) * 16, hr[i]);
  if (lane < 48) sts128(L, hcd, hcv);
  bar_lds();
  ST(25);
  produce<1>(c, top, bot, tag);
}

__global__ void __launch_bounds__(512, 2)
resblock_kernel(const u16* __restrict__ x, const u16* __restrict__ temb, const u16* __restrict__ g1,
                const u16* __restrict__ be1, const u16* __restrict__ w1, const u16* __restrict__ wemb,
                const u16* __restrict__ g2, const u16* __restrict__ be2, const u16* __restrict__ w2,
                u16* __restrict__ out, char* ws) {
  extern __shared__ __attribute__((aligned(16))) char smem[];
  Ctx c;
  c.L = (LAS char*)smem;
  c.ws = ws;
  c.tid = threadIdx.x;
  c.lane = c.tid & 63;
  c.w = __builtin_amdgcn_readfirstlane(c.tid >> 6);
  c.wg = blockIdx.x;
  const int xcd = c.wg & 7, idx = c.wg >> 3;
  c.b = xcd >> 2;
  c.ty = (xcd & 3) * 8 + (idx >> 2);
  c.tx = idx & 3;
  c.loc = c.ty * 4 + c.tx;
  c.rsX = __builtin_amdgcn_make_buffer_rsrc((void*)x, 0, X_BYTES, RSRC3);
  c.rsO = __builtin_amdgcn_make_buffer_rsrc((void*)out, 0, X_BYTES, RSRC3);
  c.rsW = __builtin_amdgcn_make_buffer_rsrc((void*)ws, 0, WS_BYTES, RSRC3);
  {
    const int lane = c.lane, w = c.w, wg = c.wg;
    ST(0);
  }
  if (c.w < 4) consumer(c, (c.loc >> 6) ? w2 : w1);
  else producer(c, c.w == 4 ? g1 : c.w == 5 ? be1 : c.w == 6 ? g2 : be2, temb, wemb);
}
}

extern "C" void kernel_launch(void* const* d_in, const int* in_sizes, int n_in, void* d_out, int out_size, void* d_ws,
                              size_t ws_size, hipStream_t stream) {
  (void)in_sizes; (void)n_in; (void)out_size; (void)ws_size;
  (void)hipFuncSetAttribute((const void*)resblock_kernel, hipFuncAttributeMaxDynamicSharedMemorySize, LDS_BYTES);
  resblock_kernel<<<256, 512, LDS_BYTES, stream>>>(
      (const u16*)d_in[0], (const u16*)d_in[1], (const u16*)d_in[2], (const u16*)d_in[3], (const u16*)d_in[4],
      (const u16*)d_in[5], (const u16*)d_in[6], (const u16*)d_in[7], (const u16*)d_in[8], (u16*)d_out, (char*)d_ws);
}
```

```cpp
#include <hip/hip_runtime.h>
#include <stdint.h>

namespace {
typedef unsigned short u16;
typedef unsigned v4u __attribute__((ext_vector_type(4)));
typedef unsigned v3u __attribute__((ext_vector_type(3)));
typedef unsigned v2u __attribute__((ext_vector_type(2)));
typedef float f32x16 __attribute__((ext_vector_type(16)));
typedef float f32x4 __attribute__((ext_vector_type(4)));
typedef float f32x2 __attribute__((ext_vector_type(2)));
typedef __bf16 bf16x8 __attribute__((ext_vector_type(8)));
typedef __bf16 bf16x2 __attribute__((ext_vector_type(2)));
#define LAS __attribute__((address_space(3)))
#define DI __device__ __forceinline__

constexpr int HSLEEP0 = 16, HSLEEP = 2, GSLEEP = 1, KSLEEP = 16;
constexpr int PRIO_S = 2, PRIO_C = 0, PRIO_P = 1;
constexpr int LOOKA = 3;
constexpr int OUTAUX = 16;

constexpr int CSV = 144, PLV = 34 * CSV;
constexpr int CSR = 112, PLR = 34 * CSR;
constexpr int L_V = 0;
constexpr int L_STG = 0;
constexpr int L_RAW = 16 * PLV;
constexpr int L_GB = L_RAW + 16 * PLR;
constexpr int L_EMB = L_GB + 2048;
constexpr int L_PART = L_EMB + 512;
constexpr int L_RED = L_PART + 256;
constexpr int L_FLG = L_RED + 192;
constexpr int L_AB = L_RED + 256;
constexpr int L_WT = L_AB + 4096;
constexpr int LDS_BYTES = L_WT + 6144;
constexpr int xk_off(int row, int ch) {
  return L_V + (8 + row * 2 + (ch >> 6)) * PLV + CSV + (row >> 1) * 64 + (ch & 63) * 64;
}
constexpr int WS_REC1 = 4096, WS_REC2 = WS_REC1 + 16384, WS_EMB = WS_REC2 + 16384;
constexpr int WS_RECW = WS_EMB + 16384;
constexpr int WS_WT = 131072, WT_WAVE = 8 * 12 * 1024, WT_CONV = 4 * WT_WAVE, WT_COPY = 2 * WT_CONV;
constexpr int WS_HALO = WS_WT + 2 * WT_COPY, HALO_WG = 24576;
constexpr int WS_XH = WS_HALO + 256 * HALO_WG;
constexpr int WS_BYTES = WS_XH + 256 * 4096;

constexpr int X_BYTES = 2 * 128 * 128 * 128 * 2;
constexpr int RSRC3 = 0x00020000;
constexpr int SC1 = 16;
constexpr int OOB = 0x40000000;
constexpr unsigned SPIN_CAP = 1u << 14;
constexpr float NLOG2E = -1.4426950408889634f;
constexpr float NLN2 = -0.6931471805599453f;
constexpr float INV_N = 1.0f / (32.0f * 128.0f * 128.0f);

typedef __amdgpu_buffer_rsrc_t rsrc_t;

DI float asf(unsigned u) { return __builtin_bit_cast(float, u); }
DI unsigned asu(float f) { return __builtin_bit_cast(unsigned, f); }
DI float bflo(unsigned u) { return __builtin_bit_cast(float, u << 16); }
DI float bfhi(unsigned u) { return __builtin_bit_cast(float, u & 0xffff0000u); }
DI unsigned cvtpk(float lo, float hi) {
  f32x2 v = {lo, hi};
  bf16x2 b = __builtin_convertvector(v, bf16x2);
  return __builtin_bit_cast(unsigned, b);
}
DI float dot2(unsigned a, unsigned b, float c) {
  return __builtin_amdgcn_fdot2_f32_bf16(__builtin_bit_cast(bf16x2, a), __builtin_bit_cast(bf16x2, b), c, false);
}
template <int CTRL> DI float dpp_add(float v) {
  int r = __builtin_amdgcn_update_dpp(0, __builtin_bit_cast(int, v), CTRL, 0xf, 0xf, true);
  return v + asf((unsigned)r);
}
DI float swap16_add(float v) {
  unsigned u = asu(v);
  auto r = __builtin_amdgcn_permlane16_swap(u, u, false, false);
  unsigned r0 = r[0], r1 = r[1];
  return asf(r0) + asf(r1);
}
DI float swap32_add(float v) {
  unsigned u = asu(v);
  auto r = __builtin_amdgcn_permlane32_swap(u, u, false, false);
  unsigned r0 = r[0], r1 = r[1];
  return asf(r0) + asf(r1);
}
DI float red_mod4(float v) {
  v = dpp_add<0x124>(v);
  v = dpp_add<0x128>(v);
  v = swap16_add(v);
  return swap32_add(v);
}
DI float red_all(float v) {
  v = dpp_add<0x121>(v);
  v = dpp_add<0x122>(v);
  return red_mod4(v);
}
DI bool all_lanes(bool ok) { return __builtin_amdgcn_ballot_w64(!ok) == 0ull; }
DI void bar() { asm volatile("s_barrier" ::: "memory"); }
DI void bar_lds() { asm volatile("s_waitcnt lgkmcnt(0)\n\ts_barrier" ::: "memory"); }
DI void drain() { asm volatile("s_waitcnt vmcnt(0)" ::: "memory"); }

DI v4u lds128(LAS char* L, int off) { return *(LAS v4u*)(L + off); }
DI void sts128(LAS char* L, int off, v4u v) { *(LAS v4u*)(L + off) = v; }
DI unsigned lds32(LAS char* L, int off) { return *(LAS unsigned*)(L + off); }
DI void sts32(LAS char* L, int off, unsigned v) { *(LAS unsigned*)(L + off) = v; }
DI f32x4 ldsf4(LAS char* L, int off) { return *(LAS f32x4*)(L + off); }
DI f32x2 ldsf2(LAS char* L, int off) { return *(LAS f32x2*)(L + off); }
DI v4u lds128a(int a) { return *(LAS v4u*)(size_t)(unsigned)a; }
DI v2u ldstr(LAS char* L, int off) {
  typedef short v4i16_t __attribute__((ext_vector_type(4)));
  return __builtin_bit_cast(v2u, __builtin_amdgcn_ds_read_tr16_b64_v4i16((LAS v4i16_t*)(L + off)));
}
DI v4u ld128(rsrc_t r, int v, int s, int) { return __builtin_amdgcn_raw_buffer_load_b128(r, v, s, 0); }
DI v4u ld128c(rsrc_t r, int v, int s) { return __builtin_amdgcn_raw_buffer_load_b128(r, v, s, SC1); }
DI unsigned ld32c(rsrc_t r, int v) { return __builtin_amdgcn_raw_buffer_load_b32(r, v, 0, SC1); }
DI void st128c(v4u d, rsrc_t r, int v) { __builtin_amdgcn_raw_buffer_store_b128(d, r, v, 0, SC1); }

DI void group_stats(LAS char* L, int off, float& mean, float& rstd) {
  f32x4 p0 = ldsf4(L, off), p1 = ldsf4(L, off + 16);
  const float S = (p0[0] + p0[2]) + (p1[0] + p1[2]), Q = (p0[1] + p0[3]) + (p1[1] + p1[3]);
  mean = S * INV_N;
  rstd = __builtin_amdgcn_rsqf(__builtin_fmaxf(Q * INV_N - mean * mean, 0.f) + 1e-5f);
}
template <int N>
DI void sigmoid_rows(const unsigned* raw, f32x2 a, f32x2 b, f32x2 (&t)[N], f32x2 (&e)[N]) {
#pragma unroll
  for (int r = 0; r < N; ++r) t[r] = f32x2{bflo(raw[r]), bfhi(raw[r])} * a + b;
#pragma unroll
  for (int r = 0; r < N; ++r) e[r] = f32x2{__builtin_amdgcn_exp2f(t[r][0]), __builtin_amdgcn_exp2f(t[r][1])} + 1.0f;
#pragma unroll
  for (int r = 0; r < N; ++r) e[r] = f32x2{__builtin_amdgcn_rcpf(e[r][0]), __builtin_amdgcn_rcpf(e[r][1])};
}
DI unsigned cvt2(f32x2 v) { return cvtpk(v[0], v[1]); }

struct Ctx {
  LAS char* L;
  char* ws;
  rsrc_t rsX, rsO, rsW;
  int tid, lane, w, wg, b, ty, tx, loc;
};

DI void transpose_rows(const Ctx& c, int ch0, int row) {
  LAS char* L = c.L;
  const int lane = c.lane, h = lane >> 5, j = lane & 31;
  const int src = L_STG + row * 8192 + (ch0 + 8 * h + ((lane & 15) >> 2)) * 64 + (((lane >> 4) & 1) * 16 + (lane & 3) * 4) * 2;
  v2u lo = ldstr(L, src), hi = ldstr(L, src + 256);
  sts128(L, L_RAW + ((ch0 >> 3) + h) * PLR + (j + 1) * CSR + row * 16, v4u{lo[0], lo[1], hi[0], hi[1]});
}

DI void gather(const Ctx& c, int off, unsigned tag, int partoff) {
  v4u a, d;
  for (unsigned spins = 0;; ++spins) {
    asm volatile("" ::: "memory");
    a = ld128c(c.rsW, off, 0);
    d = ld128c(c.rsW, off + 1024, 0);
    bool ok = (a[1] == tag) & (a[3] == tag) & (d[1] == tag) & (d[3] == tag);
    if (all_lanes(ok) || spins > SPIN_CAP) break;
    if (GSLEEP) __builtin_amdgcn_s_sleep(GSLEEP);
  }
  unsigned a0 = a[0], a2 = a[2], d0 = d[0], d2 = d[2];
  float s = red_mod4(asf(a0) + asf(d0));
  float q = red_mod4(asf(a2) + asf(d2));
  if (c.lane < 4) *(LAS f32x2*)(c.L + partoff + c.lane * 32 + c.w * 8) = f32x2{s, q};
}

template <int CONV>
DI void halo_cols(const Ctx& c, bool top, bool bot) {
  LAS char* L = c.L;
  const int lane = c.lane, pair = lane >> 5, oc = 4 * c.w + ((lane >> 3) & 3), side = (lane >> 2) & 1, pr = lane & 3;
  unsigned raw[4];
#pragma unroll
  for (int r = 0; r < 4; ++r) raw[r] = lds32(L, L_RAW + oc * PLR + (side ? 33 : 0) * CSR + (2 * pair + r) * 16 + pr * 4);
  const int ch = 8 * oc + 2 * pr;
  const f32x2 g = ldsf2(L, L_GB + CONV * 1024 + ch * 4), be = ldsf2(L, L_GB + CONV * 1024 + 512 + ch * 4);
  float mean, rstd;
  group_stats(L, L_PART + CONV * 128 + c.w * 32, mean, rstd);
  const f32x2 a = g * rstd, b = be - a * mean;
  f32x2 t[4], e[4];
  sigmoid_rows<4>(raw, a, b, t, e);
  const bool valid = side ? c.tx < 3 : c.tx > 0;
  const float k = valid ? 1.f : 0.f, k0 = (valid && !(pair == 0 && top)) ? 1.f : 0.f, k3 = (valid && !(pair == 1 && bot)) ? 1.f : 0.f;
  const f32x2 s0 = t[0] * e[0] * k0, s1 = t[1] * e[1] * k, s2 = t[2] * e[2] * k, s3 = t[3] * e[3] * k3;
  const int dst = L_V + oc * PLV + (side ? 33 : 0) * CSV + pair * 16 + pr * 4;
  sts32(L, dst, cvt2(s0 - s2));
  sts32(L, dst + 32, cvt2(s1 + s2));
  sts32(L, dst + 64, cvt2(s2 - s1));
  sts32(L, dst + 96, cvt2(s1 - s3));
}

template <int CONV>
DI void mainloop(const Ctx& c, f32x16 (&acc)[8], v4u (&wr)[12], int wbase, int wnext) {
  LAS char* L = c.L;
  const int lane = c.lane, j = lane & 31, h = lane >> 5;
  constexpr int QS = CONV == 0 ? 16 : CSV;
  int ab = (int)(size_t)L + L_V + h * PLV + (CONV == 0 ? j * CSV : (j >> 4) * 16 + (j & 15) * 2 * CSV);
  int flg = (int)(size_t)L + L_FLG;
  asm volatile("" : "+v"(ab), "+v"(flg));
  int wv[3] = {lane * 16, lane * 16 + 4096, lane * 16 + 8192};
  constexpr int LOOK = LOOKA;
  constexpr int NB = LOOK + 1;
  v4u bf[NB][2];
  bar_lds();
  __builtin_amdgcn_s_setprio(PRIO_C);
#pragma unroll
  for (int s = 0; s < LOOK; ++s)
#pragma unroll
    for (int q = 0; q < 2; ++q) bf[s][q] = lds128a(ab + q * QS + (s & 3) * 32 + (s >> 2) * CSV);
#pragma unroll 1
  for (int ck = 0; ck < 8; ck += 2) {
    asm volatile("" : "+v"(ab), "+v"(wv[0]), "+v"(wv[1]), "+v"(wv[2]));
    const int wn0 = wbase + (ck + 1) * 12288;
    const int wn1 = (ck < 6) ? wbase + (ck + 2) * 12288 : wnext;
    const bool last = CONV == 1 && ck == 6;
    int wvl[3] = {last ? OOB : wv[0], last ? OOB : wv[1], last ? OOB : wv[2]};
    asm volatile("" : "+v"(wvl[0]), "+v"(wvl[1]), "+v"(wvl[2]));
    v4u fl;
    unsigned fmin_, smin;
#pragma unroll
    for (int s2 = 0; s2 < 24; ++s2) {
      const int s = s2 % 12;
      if (s == 12 - LOOK - 5) fl = lds128a(flg);
      if (s == 12 - LOOK - 2) {
        unsigned f0 = fl[0], f1 = fl[1], f2 = fl[2], f3 = fl[3];
        fmin_ = __builtin_elementwise_min(__builtin_elementwise_min(f0, f1), __builtin_elementwise_min(f2, f3));
      }
      if (s == 12 - LOOK - 1) smin = __builtin_amdgcn_readfirstlane(fmin_);
      if (s == 12 - LOOK) {
        const unsigned need = CONV * 8 + ck + s2 / 12 + 2 - (ck + s2 / 12 == 7);
        unsigned f0, f1, f2, f3, sp;
        asm volatile(
            "s_cmp_ge_u32 %4, %7\n\t"
            "s_cbranch_scc1 2f\n\t"
            "s_movk_i32 %5, 0x4000\n"
            "1:\n\t"
            "ds_read_b32 %0, %6\n\t"
            "ds_read_b32 %1, %6 offset:4\n\t"
            "ds_read_b32 %2, %6 offset:8\n\t"
            "ds_read_b32 %3, %6 offset:12\n\t"
            "s_sub_u32 %5, %5, 1\n\t"
            "s_waitcnt lgkmcnt(0)\n\t"
            "v_min3_u32 %0, %0, %1, %2\n\t"
            "v_min_u32 %0, %0, %3\n\t"
            "s_nop 0\n\t"
            "v_readfirstlane_b32 %4, %0\n\t"
            "s_nop 3\n\t"
            "s_cmp_ge_u32 %4, %7\n\t"
            "s_cbranch_scc1 2f\n\t"
            "s_cmp_lg_u32 %5, 0\n\t"
            "s_cbranch_scc1 1b\n"
            "2:"
            : "=&v"(f0), "=&v"(f1), "=&v"(f2), "=&v"(f3), "+s"(smin), "=&s"(sp)
            : "v"(flg), "s"(need)
            : "memory", "scc");
      }
      if ((s & 1) == 0) {
        __builtin_amdgcn_s_waitcnt(0x027A);
        __builtin_amdgcn_sched_barrier(0);
      }
      const int sn = (s + LOOK) % 12;
      const int cn = ((s2 + LOOK) / 12) * 2 * PLV;
#pragma unroll
      for (int q = 0; q < 2; ++q) {
        bf[(s + LOOK) % NB][q] = lds128a(ab + cn + q * QS + (sn & 3) * 32 + (sn >> 2) * CSV);
        acc[(s & 3) * 2 + q] = __builtin_amdgcn_mfma_f32_32x32x16_bf16(
            __builtin_bit_cast(bf16x8, wr[s]), __builtin_bit_cast(bf16x8, bf[s % NB][q]), acc[(s & 3) * 2 + q], 0, 0, 0);
        __builtin_amdgcn_sched_barrier(0);
      }
      wr[s] = ld128c(c.rsW, (s2 < 12 ? wv[s >> 2] : wvl[s >> 2]) + (s & 3) * 1024, s2 < 12 ? wn0 : wn1);
      __builtin_amdgcn_sched_barrier(0);
    }
    ab += 4 * PLV;
  }
}

template <int CONV, int C0, int C1>
DI void produce_loop(int src, int dst, int abo, int flg, int edge, float ktop, float kbot) {
  unsigned fval = CONV * 8 + C0;
  int cnt;
  asm volatile(
      "ds_read2_b32 v[104:105], %[src] offset1:4\n\t"
      "ds_read2_b32 v[106:107], %[src] offset0:8 offset1:12\n\t"
      "ds_read2_b32 v[108:109], %[src] offset0:16 offset1:20\n\t"
      "ds_read_b128 v[100:103], %[abo]\n\t"
      "s_mov_b32 %[cnt], %[c0]\n\t"
      ".if %c[c0] == 1\n\t"
      "s_waitcnt lgkmcnt(4)\n\t"
      "s_barrier\n\t"
      ".endif\n\t"
      "s_waitcnt lgkmcnt(0)\n"
      "1:\n\t"
      "s_waitcnt lgkmcnt(4)\n\t"
      "v_add_u32 %[src], 0x1dc0, %[src]\n\t"
      "v_add_u32 %[abo], 0x80, %[abo]\n\t"
      "v_lshlrev_b32 v66, 16, v105\n\t"
      "v_and_b32 v67, 0xffff0000, v105\n\t"
      "v_fma_f32 v78, v66, v100, v102\n\t"
      "v_lshlrev_b32 v68, 16, v106\n\t"
      "v_exp_f32 v90, v78\n\t"
      "v_fma_f32 v79, v67, v101, v103\n\t"
      "v_and_b32 v69, 0xffff0000, v106\n\t"
      "v_exp_f32 v91, v79\n\t"
      "v_fma_f32 v80, v68, v100, v102\n\t"
      "v_fma_f32 v81, v69, v101, v103\n\t"
      "v_exp_f32 v92, v80\n\t"
      "v_lshlrev_b32 v70, 16, v107\n\t"
      "v_and_b32 v71, 0xffff0000, v107\n\t"
      "v_exp_f32 v93, v81\n\t"
      "v_fma_f32 v82, v70, v100, v102\n\t"
      "v_lshlrev_b32 v72, 16, v108\n\t"
      "v_exp_f32 v94, v82\n\t"
      "v_fma_f32 v83, v71, v101, v103\n\t"
      "v_and_b32 v73, 0xffff0000, v108\n\t"
      "v_exp_f32 v95, v83\n\t"
      "v_fma_f32 v84, v72, v100, v102\n\t"
      "v_fma_f32 v85, v73, v101, v103\n\t"
      "v_exp_f32 v96, v84\n\t"
      "v_lshlrev_b32 v64, 16, v104\n\t"
      "v_and_b32 v65, 0xffff0000, v104\n\t"
      "v_exp_f32 v97, v85\n\t"
      "ds_read2_b32 v[104:105], %[src] offset1:4\n\t"
      "v_fma_f32 v76, v64, v100, v102\n\t"
      "v_lshlrev_b32 v74, 16, v109\n\t"
      "v_exp_f32 v88, v76\n\t"
      "ds_read2_b32 v[106:107], %[src] offset0:8 offset1:12\n\t"
      "v_fma_f32 v77, v65, v101, v103\n\t"
      "v_add_f32 v90, 1.0, v90\n\t"
      "v_exp_f32 v89, v77\n\t"
      "v_fma_f32 v86, v74, v100, v102\n\t"
      "v_and_b32 v75, 0xffff0000, v109\n\t"
      "v_exp_f32 v98, v86\n\t"
      "ds_read2_b32 v[108:109], %[src] offset0:16 offset1:20\n\t"
      "v_fma_f32 v87, v75, v101, v103\n\t"
      "v_rcp_f32 v90, v90\n\t"
      "s_waitcnt lgkmcnt(3)\n\t"
      "s_mov_b64 exec, 1\n\t"
      "ds_write_b32 %[flg], %[fval]\n\t"
      "s_mov_b64 exec, -1\n\t"
      "v_add_u32 %[fval], 1, %[fval]\n\t"
      "ds_read_b128 v[100:103], %[abo]\n\t"
      "v_exp_f32 v99, v87\n\t"
      "v_add_f32 v91, 1.0, v91\n\t"
      "v_add_f32 v92, 1.0, v92\n\t"
      "v_rcp_f32 v91, v91\n\t"
      "v_add_f32 v93, 1.0, v93\n\t"
      "v_add_f32 v94, 1.0, v94\n\t"
      "v_rcp_f32 v92, v92\n\t"
      "v_add_f32 v95, 1.0, v95\n\t"
      "v_add_f32 v96, 1.0, v96\n\t"
      "v_rcp_f32 v93, v93\n\t"
      "v_add_f32 v97, 1.0, v97\n\t"
      "v_add_f32 v88, 1.0, v88\n\t"
      "v_rcp_f32 v94, v94\n\t"
      "v_add_f32 v89, 1.0, v89\n\t"
      "v_add_f32 v98, 1.0, v98\n\t"
      "v_rcp_f32 v95, v95\n\t"
      "v_add_f32 v99, 1.0, v99\n\t"
      "v_mul_f32 v82, v82, v94\n\t"
      "v_rcp_f32 v96, v96\n\t"
      "v_mul_f32 v78, v78, v90\n\t"
      "v_mul_f32 v81, v81, v93\n\t"
      "v_rcp_f32 v97, v97\n\t"
      "v_mul_f32 v84, v84, v96\n\t"
      "v_mul_f32 v80, v80, v92\n\t"
      "v_rcp_f32 v88, v88\n\t"
      "v_mul_f32 v83, v83, v95\n\t"
      "v_mul_f32 v85, v85, v97\n\t"
      "v_rcp_f32 v89, v89\n\t"
      "v_mul_f32 v79, v79, v91\n\t"
      "v_add_f32 v90, v78, v80\n\t"
      "v_rcp_f32 v98, v98\n\t"
      "v_add_f32 v91, v79, v81\n\t"
      "v_sub_f32 v92, v80, v78\n\t"
      "v_rcp_f32 v99, v99\n\t"
      "v_sub_f32 v96, v80, v84\n\t"
      "v_sub_f32 v93, v81, v79\n\t"
      "s_cmp_lg_u32 %[edge], 0\n\t"
      "s_cbranch_scc0 3f\n\t"
      "v_mul_f32 v88, %[ktop], v88\n\t"
      "v_mul_f32 v89, %[ktop], v89\n\t"
      "v_mul_f32 v98, %[kbot], v98\n\t"
      "v_mul_f32 v99, %[kbot], v99\n\t"
      "s_nop 0\n\t"
      "3:\n\t"
      "v_sub_f32 v95, v79, v83\n\t"
      "v_sub_f32 v94, v78, v82\n\t"
      "v_sub_f32 v97, v81, v85\n\t"
      "v_fma_f32 v98, -v86, v98, v82\n\t"
      "v_fma_f32 v88, v76, v88, -v80\n\t"
      "v_fma_f32 v99, -v87, v99, v83\n\t"
      "v_fma_f32 v89, v77, v89, -v81\n\t"
      "v_sub_f32 v86, v84, v82\n\t"
      "v_sub_f32 v87, v85, v83\n\t"
      "v_add_f32 v77, v83, v85\n\t"
      "v_add_f32 v76, v82, v84\n\t"
      "v_cvt_pk_bf16_f32 v98, v98, v99\n\t"
      "v_cvt_pk_bf16_f32 v96, v96, v97\n\t"
      "v_cvt_pk_bf16_f32 v76, v76, v77\n\t"
      "v_cvt_pk_bf16_f32 v90, v90, v91\n\t"
      "ds_write2_b32 %[dst], v90, v76 offset0:8 offset1:12\n\t"
      "v_cvt_pk_bf16_f32 v88, v88, v89\n\t"
      "ds_write2_b32 %[dst], v88, v96 offset0:0 offset1:4\n\t"
      "v_cvt_pk_bf16_f32 v92, v92, v93\n\t"
      "v_cvt_pk_bf16_f32 v94, v94, v95\n\t"
      "ds_write2_b32 %[dst], v94, v98 offset0:24 offset1:28\n\t"
      "v_cvt_pk_bf16_f32 v86, v86, v87\n\t"
      "ds_write2_b32 %[dst], v92, v86 offset0:16 offset1:20\n\t"
      "v_add_u32 %[dst], 0x2640, %[dst]\n\t"
      "s_add_u32 %[cnt], %[cnt], 1\n\t"
      "s_cmp_lt_u32 %[cnt], %[c1]\n\t"
      "s_cbranch_scc1 1b\n\t"
      "s_waitcnt lgkmcnt(0)"
      : [src] "+v"(src), [dst] "+v"(dst), [abo] "+v"(abo), [fval] "+v"(fval), [cnt] "=&s"(cnt)
      : [flg] "v"(flg), [edge] "s"(edge), [ktop] "s"(ktop), [kbot] "s"(kbot), [c0] "i"(C0), [c1] "i"(C1)
      : "v64", "v65", "v66", "v67", "v68", "v69", "v70", "v71", "v72", "v73", "v74", "v75",
        "v76", "v77", "v78", "v79", "v80", "v81", "v82", "v83", "v84", "v85", "v86", "v87",
        "v88", "v89", "v90", "v91", "v92", "v93", "v94", "v95", "v96", "v97", "v98", "v99",
        "v100", "v101", "v102", "v103", "v104", "v105", "v106", "v107", "v108", "v109", "scc", "memory");
}

template <int CONV>
DI void produce(const Ctx& c, bool top, bool bot, unsigned tag) {
  LAS char* L = c.L;
  const int lane = c.lane, p = c.w - 4;
  const int o = lane >> 5, col = 8 * p + ((lane >> 2) & 7), pr = lane & 3;
  static_assert(2 * PLR == 0x1dc0 && 2 * PLV == 0x2640, "strides inside produce_loop");
  const int src = L_RAW + o * PLR + (col + 1) * CSR + pr * 4, dst = L_V + o * PLV + (col + 1) * CSV + pr * 4;
  unsigned raw[6];
#pragma unroll
  for (int r = 0; r < 6; ++r) raw[r] = lds32(L, src + r * 16);
  const int gb = L_GB + CONV * 1024;
  const f32x2 g0 = ldsf2(L, gb + (8 * o + 2 * pr) * 4), be0 = ldsf2(L, gb + 512 + (8 * o + 2 * pr) * 4);
  const f32x2 gt = ldsf2(L, gb + lane * 8), bt = ldsf2(L, gb + 512 + lane * 8);
  float mean0, rstd0, meant, rstdt;
  group_stats(L, L_PART + CONV * 128, mean0, rstd0);
  group_stats(L, L_PART + CONV * 128 + (lane >> 4) * 32, meant, rstdt);
  {
    const f32x2 a = g0 * rstd0, b = be0 - a * mean0;
    f32x2 t[6], e[6];
    sigmoid_rows<6>(raw, a, b, t, e);
    const f32x2 s0 = t[0] * e[0] * (top ? 0.f : 1.f), s1 = t[1] * e[1], s2 = t[2] * e[2], s3 = t[3] * e[3];
    const f32x2 s4 = t[4] * e[4], s5 = t[5] * e[5] * (bot ? 0.f : 1.f);
    sts32(L, dst, cvt2(s0 - s2));
    sts32(L, dst + 16, cvt2(s2 - s4));
    sts32(L, dst + 32, cvt2(s1 + s2));
    sts32(L, dst + 48, cvt2(s3 + s4));
    sts32(L, dst + 64, cvt2(s2 - s1));
    sts32(L, dst + 80, cvt2(s4 - s3));
    sts32(L, dst + 96, cvt2(s1 - s3));
    sts32(L, dst + 112, cvt2(s3 - s5));
    const f32x2 at = gt * rstdt, btt = bt - at * meant;
    *(LAS f32x4*)(L + L_AB + p * 1024 + lane * 16) = f32x4{at[0], at[1], btt[0], btt[1]};
  }
  const int lb = (int)(size_t)L;
  const int edge = __builtin_amdgcn_readfirstlane((top || bot) ? 1 : 0);
  const float ktop = asf(__builtin_amdgcn_readfirstlane(top ? 0u : 0x3f800000u));
  const float kbot = asf(__builtin_amdgcn_readfirstlane(bot ? 0u : 0x3f800000u));
  const int abo = lb + L_AB + p * 1024 + (4 * o + pr) * 16, flg = lb + L_FLG + p * 4;
  v4u eg[8];
  const int ego = WS_EMB + (c.b * 128 + (lane >> 2)) * 64 + (lane & 3) * 16;
  if (CONV == 0 && p == 3) {
    produce_loop<CONV, 1, 4>(lb + src + 2 * PLR, lb + dst + 2 * PLV, abo + 128, flg, edge, ktop, kbot);
#pragma unroll
    for (int kr = 0; kr < 8; ++kr) eg[kr] = ld128c(c.rsW, ego + kr * 1024, 0);
    produce_loop<CONV, 4, 8>(lb + src + 8 * PLR, lb + dst + 8 * PLV, abo + 512, flg, edge, ktop, kbot);
  } else {
    produce_loop<CONV, 1, 8>(lb + src + 2 * PLR, lb + dst + 2 * PLV, abo + 128, flg, edge, ktop, kbot);
  }
  if (CONV == 0 && p == 3) {
    for (unsigned spins = 0;; ++spins) {
      bool ok = true;
      float e0 = 0.f, e1 = 0.f;
#pragma unroll
      for (int kr = 0; kr < 8; ++kr) {
        unsigned g0_ = eg[kr][0], g2_ = eg[kr][2];
        ok = ok & (eg[kr][1] == tag) & (eg[kr][3] == tag);
        e0 += asf(g0_);
        e1 += asf(g2_);
      }
      *(LAS f32x2*)(L + L_EMB + lane * 8) = f32x2{e0, e1};
      if (all_lanes(ok) || spins > SPIN_CAP) break;
      asm volatile("" ::: "memory");
#pragma unroll
      for (int kr = 0; kr < 8; ++kr) eg[kr] = ld128c(c.rsW, ego + kr * 1024, 0);
    }
    asm volatile("s_waitcnt lgkmcnt(0)" ::: "memory");
  }
  if (lane == 0) *(LAS unsigned*)(L + L_FLG + p * 4) = CONV * 8 + 8;
}

DI void publish(const Ctx& c, int rec, float s, float q, unsigned tag) {
  st128c(v4u{asu(s), tag, asu(q), tag}, c.rsW, rec + (c.b * 128 + c.loc) * 64 + c.lane * 16);
}
DI bool tags_ok(v4u g0, v4u g1, unsigned tag) {
  return (g0[1] == tag) & (g0[3] == tag) & (g1[1] == tag) & (g1[3] == tag);
}

struct P1 {
  unsigned xold;
  v3u d0, d1, d2;
  v4u xi[6];
};
DI void phase1_loads(const Ctx& c, const u16* wsrc, P1& f) {
  const int lane = c.lane, w = c.w, b = c.b, ty = c.ty, tx = c.tx, loc = c.loc;
  f.xold = __builtin_amdgcn_raw_buffer_load_b32(c.rsW, b * 256, 0, 0);
  const int off = ((b * 128 + 16 * w + (lane >> 2)) * 128 + ty * 4) * 256 + tx * 64 + (lane & 3) * 16;
  {
    asm volatile("" : "+s"(wsrc));
    const rsrc_t rs = __builtin_amdgcn_make_buffer_rsrc((void*)wsrc, 0, 128 * 128 * 9 * 2, RSRC3);
    const int wn = (loc >> 4) & 3, wc = (loc >> 1) & 7, woh = loc & 1, oo = lane >> 2, pr = lane & 3;
    const int offw = (w >> 1) == 1 ? ((32 * wn + 16 * woh + oo) * 128 + 16 * wc + 8 * (w & 1) + 2 * pr) * 18 : OOB;
    f.d0 = __builtin_amdgcn_raw_buffer_load_b96(rs, offw, 0, 0);
    f.d1 = __builtin_amdgcn_raw_buffer_load_b96(rs, offw + 12, 0, 0);
    f.d2 = __builtin_amdgcn_raw_buffer_load_b96(rs, offw + 24, 0, 0);
    asm volatile("" ::: "memory");
  }
#pragma unroll
  for (int r = 0; r < 4; ++r) f.xi[r + 1] = ld128(c.rsX, off + r * 256, 0, 0);
  f.xi[0] = ld128(c.rsX, ty == 0 ? OOB : off - 256, 0, 0);
  f.xi[5] = ld128(c.rsX, ty == 31 ? OOB : off + 1024, 0, 0);
  asm volatile("" ::: "memory");
}
template <bool WEIGHTS>
DI unsigned phase1(const Ctx& c, P1& f) {
  LAS char* L = c.L;
  const int lane = c.lane, w = c.w, b = c.b, loc = c.loc;
  const int wn = (loc >> 4) & 3, wc = (loc >> 1) & 7, woh = loc & 1, oo = lane >> 2, pr = lane & 3;
  const unsigned xold = f.xold;
  const v3u d0 = f.d0, d1 = f.d1, d2 = f.d2;
  v4u (&xi)[6] = f.xi;
  float s = 0.f, q = 0.f;
#pragma unroll
  for (int r = 1; r < 5; ++r)
#pragma unroll
    for (int d = 0; d < 4; ++d) {
      unsigned u = xi[r][d];
      s = dot2(u, 0x3f803f80u, s);
      q = dot2(u, u, q);
    }
  s = red_all(s);
  q = red_all(q);
  const unsigned tag = __builtin_amdgcn_readfirstlane(xold) * 0x9E3779B1u + 0x7F4A7C15u;
  if (lane == 0) sts128(L, L_RED + w * 16, v4u{asu(s), asu(q), tag, 0u});
  if (c.w == 0) {
    v4u v;
    for (unsigned spins = 0;; ++spins) {
      asm volatile("" ::: "memory");
      v = lds128(L, L_RED + (lane & 7) * 16);
      if (all_lanes(v[2] == tag) || spins > (SPIN_CAP << 4)) break;
    }
    unsigned v0 = v[0], v1 = v[1];
    float ps = dpp_add<0xb1>(asf(v0)), pq = dpp_add<0xb1>(asf(v1));
    int off = WS_REC1 + (b * 128 + loc) * 64 + (lane >> 1) * 16;
    if (lane >= 8 || (lane & 1)) off = OOB;
    st128c(v4u{asu(ps), tag, asu(pq), tag}, c.rsW, off);
  }
  {
    const int sd = (lane & 3) == 3;
    const bool act = (lane & 7) == 0 || (lane & 7) == 3;
#pragma unroll
    for (int r = 0; r < 4; ++r) {
      const unsigned v = sd ? (xi[r + 1][3] >> 16) : (xi[r + 1][0] & 0xffffu);
      const unsigned nb = (unsigned)__builtin_amdgcn_update_dpp(0, (int)v, 0x104, 0xf, 0xf, true);
      int off = WS_XH + (b * 128 + loc) * 4096 + ((sd * 4 + r) * 16 + 2 * w + (lane >> 5)) * 32 + ((lane >> 3) & 3) * 8;
      if (!act) off = OOB;
      __builtin_amdgcn_raw_buffer_store_b64(v2u{v | (nb << 16), tag}, c.rsW, off, 0, SC1);
    }
  }
  if (WEIGHTS) {
    const unsigned d[9] = {d0[0], d0[1], d0[2], d1[0], d1[1], d1[2], d2[0], d2[1], d2[2]};
    float e[18];
#pragma unroll
    for (int i = 0; i < 9; ++i) { e[2 * i] = bflo(d[i]); e[2 * i + 1] = bfhi(d[i]); }
    const int wh = w & 1, sc = L_WT + wh * 12 * 256 + lane * 4;
#pragma unroll
    for (int dx = 0; dx < 3; ++dx) {
      float a0 = e[dx], a1 = e[3 + dx], a2 = e[6 + dx], b0 = e[9 + dx], b1 = e[12 + dx], b2 = e[15 + dx];
      float ta = a0 + a2, tb = b0 + b2;
      sts32(L, sc + (dx * 4 + 0) * 256, cvtpk(a0 * NLN2, b0 * NLN2));
      sts32(L, sc + (dx * 4 + 1) * 256, cvtpk((ta + a1) * (0.5f * NLN2), (tb + b1) * (0.5f * NLN2)));
      sts32(L, sc + (dx * 4 + 2) * 256, cvtpk((ta - a1) * (0.5f * NLN2), (tb - b1) * (0.5f * NLN2)));
      sts32(L, sc + (dx * 4 + 3) * 256, cvtpk(a2 * NLN2, b2 * NLN2));
    }
    const int dst = WS_WT + b * WT_COPY + (loc >> 6) * WT_CONV + wn * WT_WAVE + wc * 12288 + (wh * 32 + woh * 16 + oo) * 16;
#pragma unroll
    for (int g = 0; g < 3; ++g) {
      v4u o = lds128(L, L_WT + wh * 12 * 256 + (4 * g + pr) * 256 + oo * 16);
      st128c(o, c.rsW, dst + (4 * g + pr) * 1024);
    }
  }
  if (WEIGHTS) {
    drain();
    if (lane == 0) sts32(L, L_RED + 176 + (w & 1) * 4, tag);
    if (w == 3) {
      for (unsigned spins = 0;; ++spins) {
        asm volatile("" ::: "memory");
        if (all_lanes(lds32(L, L_RED + 176 + (lane & 1) * 4) == tag) || spins > (SPIN_CAP << 4)) break;
      }
      if (lane == 0) __builtin_amdgcn_raw_buffer_store_b64(v2u{tag, tag}, c.rsW, WS_RECW + (b * 128 + loc) * 64, 0, SC1);
    }
  }
#pragma unroll
  for (int r = 0; r < 6; ++r) sts128(L, L_STG + r * 8192 + w * 1024 + lane * 16, xi[r]);
#pragma unroll
  for (int r = 0; r < 6; ++r) transpose_rows(c, 16 * w, r);
  return tag;
}

DI void consumer(const Ctx& c, P1& f) {
  LAS char* L = c.L;
  const int lane = c.lane, w = c.w, b = c.b, ty = c.ty, tx = c.tx, loc = c.loc;
  const unsigned tag = w >= 2 ? phase1<true>(c, f) : phase1<false>(c, f);
  __builtin_amdgcn_s_setprio(PRIO_S);
  gather(c, WS_REC1 + b * 8192 + w * 2048 + lane * 16, tag, L_PART);
  const int wo = WS_RECW + (b * 128 + lane) * 64;
  unsigned t0 = ld32c(c.rsW, wo), t1 = ld32c(c.rsW, wo + 4096);
  bar_lds();
  const int wb1 = WS_WT + b * WT_COPY + w * WT_WAVE, wb2 = wb1 + WT_CONV;
  const bool top = ty == 0, bot = ty == 31;
  v4u wr[12];
  if (all_lanes((t0 == tag) & (t1 == tag))) {
#pragma unroll
    for (int s = 0; s < 12; ++s) wr[s] = ld128c(c.rsW, lane * 16, wb1 + s * 1024);
    halo_cols<0>(c, top, bot);
  } else {
    halo_cols<0>(c, top, bot);
    for (unsigned spins = 0; spins <= SPIN_CAP; ++spins) {
      asm volatile("" ::: "memory");
      if (all_lanes((ld32c(c.rsW, wo) == tag) & (ld32c(c.rsW, wo + 4096) == tag))) break;
    }
#pragma unroll
    for (int s = 0; s < 12; ++s) wr[s] = ld128c(c.rsW, lane * 16, wb1 + s * 1024);
  }
  f32x16 acc[8];
#pragma unroll
  for (int m = 0; m < 8; ++m)
#pragma unroll
    for (int r = 0; r < 16; ++r) acc[m][r] = 0.f;
  mainloop<0>(c, acc, wr, wb1, wb2);
  const int j = lane & 31, h = lane >> 5;
  {
    f32x4 ev[4];
#pragma unroll
    for (int k = 0; k < 4; ++k) ev[k] = ldsf4(L, L_EMB + (32 * w + 8 * k + 4 * h) * 4);
    float s = 0.f, q = 0.f;
    v4u hv[8];
    const int hb = WS_HALO + (b * 128 + loc) * HALO_WG;
    const int ldst = L_RAW + (4 * w + h) * PLR + (j + 1) * CSR + 16;
#pragma unroll
    for (int mi = 0; mi < 4; ++mi) {
      const int m = mi == 0 ? 0 : mi == 1 ? 3 : mi - 1;
      const int pq = m >> 1;
      unsigned pk[4][2];
#pragma unroll
      for (int k = 0; k < 4; ++k) {
        f32x2 v[2];
#pragma unroll
        for (int i = 0; i < 2; ++i) {
          const int r = 4 * k + 2 * i;
          f32x2 m0 = {acc[0 + pq][r], acc[0 + pq][r + 1]}, m1 = {acc[2 + pq][r], acc[2 + pq][r + 1]};
          f32x2 m2 = {acc[4 + pq][r], acc[4 + pq][r + 1]}, m3 = {acc[6 + pq][r], acc[6 + pq][r + 1]};
          f32x2 ee = {ev[k][2 * i], ev[k][2 * i + 1]};
          v[i] = (m & 1) ? ((m1 + ee) - m2) - m3 : ((m1 + ee) + m2) + m0;
        }
        pk[k][0] = cvt2(v[0]);
        pk[k][1] = cvt2(v[1]);
      }
#pragma unroll
      for (int t = 0; t < 2; ++t) {
        auto r0 = __builtin_amdgcn_permlane32_swap(pk[2 * t][0], pk[2 * t + 1][0], false, false);
        auto r1 = __builtin_amdgcn_permlane32_swap(pk[2 * t][1], pk[2 * t + 1][1], false, false);
        v4u o = {r0[0], r1[0], r0[1], r1[1]};
        if (m == 0 || m == 3) st128c(o, c.rsW, hb + (m == 0 ? 0 : 8192) + (w * 2 + t) * 1024 + lane * 16);
        sts128(L, ldst + 2 * t * PLR + m * 16, o);
        hv[mi * 2 + t] = o;
      }
      __builtin_amdgcn_sched_barrier(0);
    }
    {
      const int oc = 4 * w + (lane & 3), sd = (lane >> 2) & 1, mi = (lane >> 3) & 1;
      const v4u d = lds128(L, L_RAW + oc * PLR + (sd ? 32 : 1) * CSR + (mi + 2) * 16);
      st128c(d, c.rsW, lane < 16 ? hb + 16384 + (oc >> 1) * 1024 + ((sd ? 4 : 0) + mi * 2 + (oc & 1)) * 16 : OOB);
    }
#pragma unroll
    for (int i = 0; i < 8; ++i)
#pragma unroll
      for (int d = 0; d < 4; ++d) {
        const unsigned u = hv[i][d];
        s = dot2(u, 0x3f803f80u, s);
        q = dot2(u, u, q);
      }
    s = red_all(s);
    q = red_all(q);
    if (lane == 0) *(LAS f32x2*)(L + L_RED + 128 + w * 8) = f32x2{s, q};
  }
  drain();
  bar_lds();
  if (w == 0 && lane < 4) {
    f32x2 v = ldsf2(L, L_RED + 128 + lane * 8);
    publish(c, WS_REC2, v[0], v[1], tag);
  }
  bar();
  {
    const int xo = xk_off(2 * (j >> 4), 32 * w + 4 * h) + (j & 15) * 4;
#pragma unroll
    for (int r = 0; r < 16; ++r) {
      const unsigned u0 = lds32(L, xo + ((r & 3) + 8 * (r >> 2)) * 64);
      const unsigned u1 = lds32(L, xo + 2 * PLV + ((r & 3) + 8 * (r >> 2)) * 64);
      acc[0][r] = bflo(u0);
      acc[1][r] = bfhi(u0);
      acc[6][r] = -bflo(u1);
      acc[7][r] = -bfhi(u1);
      acc[2][r] = 0.f; acc[3][r] = 0.f; acc[4][r] = 0.f; acc[5][r] = 0.f;
    }
  }
  __builtin_amdgcn_s_setprio(PRIO_S);
  gather(c, WS_REC2 + b * 8192 + w * 2048 + lane * 16, tag, L_PART + 128);
  bar_lds();
  halo_cols<1>(c, top, bot);
  mainloop<1>(c, acc, wr, wb2, wb2);
  {
    const int ooff = ((b * 128 + 32 * w + 4 * h) * 128 + ty * 4 + 2 * (j >> 4)) * 256 + tx * 64 + (j & 15) * 4;
#pragma unroll
    for (int r = 0; r < 16; r += 2) {
      f32x2 y0[2], y1[2];
#pragma unroll
      for (int q = 0; q < 2; ++q) {
        f32x2 m0 = {acc[0 + q][r], acc[0 + q][r + 1]}, m1 = {acc[2 + q][r], acc[2 + q][r + 1]};
        f32x2 m2 = {acc[4 + q][r], acc[4 + q][r + 1]}, m3 = {acc[6 + q][r], acc[6 + q][r + 1]};
        y0[q] = (m1 + m2) + m0;
        y1[q] = (m1 - m2) - m3;
      }
#pragma unroll
      for (int i = 0; i < 2; ++i) {
        const int so = (((r + i) & 3) + 8 * ((r + i) >> 2)) * 32768;
        __builtin_amdgcn_raw_buffer_store_b32(cvtpk(y0[0][i], y0[1][i]), c.rsO, ooff, so, OUTAUX);
        __builtin_amdgcn_raw_buffer_store_b32(cvtpk(y1[0][i], y1[1][i]), c.rsO, ooff + 256, so, OUTAUX);
      }
    }
  }
}

DI void producer(const Ctx& c, P1& f, const u16* gtab, const u16* temb, const u16* wemb) {
  LAS char* L = c.L;
  const int lane = c.lane, w = c.w, b = c.b, ty = c.ty, tx = c.tx, loc = c.loc, p = w - 4;
  const unsigned tb0 = gtab[lane], tb1 = gtab[lane + 64];
  unsigned te0 = 0, te1 = 0;
  v4u we0 = {0u, 0u, 0u, 0u}, we1 = {0u, 0u, 0u, 0u};
  if (p == 3) {
    const int k0 = 96 * (loc >> 4) + lane, k1 = k0 + 64;
    te0 = temb[b * 768 + k0];
    we0 = *(const v4u*)(wemb + k0 * 128 + (loc & 15) * 8);
    if (lane < 32) {
      te1 = temb[b * 768 + k1];
      we1 = *(const v4u*)(wemb + k1 * 128 + (loc & 15) * 8);
    }
  }
  const unsigned tag = phase1<false>(c, f);
  if (p == 3) {
    float t0 = asf(te0 << 16), t1 = asf(te1 << 16);
    float s0 = t0 * __builtin_amdgcn_rcpf(1.0f + __builtin_amdgcn_exp2f(t0 * NLOG2E));
    float s1 = t1 * __builtin_amdgcn_rcpf(1.0f + __builtin_amdgcn_exp2f(t1 * NLOG2E));
    s0 = bflo(cvtpk(s0, s0));
    s1 = bflo(cvtpk(s1, s1));
    float pe[8];
#pragma unroll
    for (int d = 0; d < 4; ++d) {
      unsigned u0 = we0[d], u1 = we1[d];
      pe[2 * d] = red_all(s0 * bflo(u0) + s1 * bflo(u1));
      pe[2 * d + 1] = red_all(s0 * bfhi(u0) + s1 * bfhi(u1));
    }
    const int q = lane & 3;
    float va = q == 0 ? pe[0] : q == 1 ? pe[2] : q == 2 ? pe[4] : pe[6];
    float vb = q == 0 ? pe[1] : q == 1 ? pe[3] : q == 2 ? pe[5] : pe[7];
    if (lane < 4) publish(c, WS_EMB, va, vb, tag);
  }
  *(LAS float*)(L + L_GB + p * 512 + lane * 4) = asf(tb0 << 16) * NLOG2E;
  *(LAS float*)(L + L_GB + p * 512 + 256 + lane * 4) = asf(tb1 << 16) * NLOG2E;
  const bool top = ty == 0, bot = ty == 31;
  const int hq = lane % 12, hoc = 4 * p + lane / 12, hrr = hq >> 1, hsd = hq & 1;
  const int hnl = b * 128 + loc + (hsd ? 1 : -1) + (hrr == 0 ? -4 : hrr == 5 ? 4 : 0);
  const bool hneed = lane < 48 && !(hsd ? tx == 3 : tx == 0) && !(hrr == 0 && top) && !(hrr == 5 && bot);
  const int hdst = L_RAW + hoc * PLR + (hsd ? 33 : 0) * CSR + hrr * 16;
  {
    const int nrow = hrr == 0 ? 3 : hrr == 5 ? 0 : hrr - 1;
    int off = WS_XH + hnl * 4096 + (((hsd ? 0 : 1) * 4 + nrow) * 16 + hoc) * 32;
    if (!hneed) off = OOB;
    v4u g0, g1;
    __builtin_amdgcn_s_sleep(HSLEEP0);
    for (unsigned spins = 0;; ++spins) {
      asm volatile("" ::: "memory");
      g0 = ld128c(c.rsW, off, 0);
      g1 = ld128c(c.rsW, off + 16, 0);
      if (all_lanes(!hneed || tags_ok(g0, g1, tag)) || spins > SPIN_CAP) break;
      __builtin_amdgcn_s_sleep(HSLEEP);
    }
    if (lane < 48) sts128(L, hdst, v4u{g0[0], g0[2], g1[0], g1[2]});
  }
  if (lane == 0) *(LAS unsigned*)(L + L_FLG + p * 4) = 0u;
  __builtin_amdgcn_s_setprio(PRIO_P);
  bar_lds();
  if (p == 3 && lane == 0 && loc == 0) __builtin_amdgcn_raw_buffer_store_b32(tag, c.rsW, b * 256, 0, SC1);
  produce<0>(c, top, bot, tag);
  __builtin_amdgcn_s_sleep(KSLEEP);
  v4u keep[8];
#pragma unroll
  for (int i = 0; i < 8; ++i)
    keep[i] = ld128(c.rsX, ((b * 128 + 32 * p + (lane >> 2)) * 128 + ty * 4 + (i & 3)) * 256 + tx * 64 + (lane & 3) * 16,
                    (i >> 2) * 16 * 32768, 0);
  bar();
#pragma unroll
  for (int i = 0; i < 8; ++i) sts128(L, xk_off(i & 3, 32 * p + 16 * (i >> 2)) + lane * 16, keep[i]);
  bar_lds();
  {
    const int dy = lane < 3 ? -1 : lane < 5 ? 0 : 1;
    const int dxx = lane == 0 || lane == 3 || lane == 5 ? -1 : lane == 1 || lane == 6 ? 0 : 1;
    const int ny = ty + dy, nx = tx + dxx;
    int off = WS_REC2 + (b * 128 + ny * 4 + nx) * 64 + 4;
    const bool need = lane < 8 && (unsigned)ny < 32u && (unsigned)nx < 4u;
    if (!need) off = OOB;
    for (unsigned spins = 0;; ++spins) {
      asm volatile("" ::: "memory");
      unsigned t = ld32c(c.rsW, off);
      if (all_lanes(!need || t == tag) || spins > SPIN_CAP) break;
      __builtin_amdgcn_s_sleep(HSLEEP);
    }
    v4u hr[4];
#pragma unroll
    for (int i = 0; i < 4; ++i) {
      int o2 = WS_HALO + (b * 128 + loc + ((i & 1) ? 4 : -4)) * HALO_WG + ((i & 1) ? 0 : 8192) + (2 * p + (i >> 1)) * 1024 + lane * 16;
      if ((i & 1) ? bot : top) o2 = OOB;
      hr[i] = ld128c(c.rsW, o2, 0);
    }
    const int base = WS_HALO + hnl * HALO_WG + (hoc >> 1) * 1024, col = hsd ? 0 : 31, hh = hoc & 1;
    int offc;
    if (hrr == 0 || hrr == 4) offc = base + 8192 + (hh * 32 + col) * 16;
    else if (hrr == 5 || hrr == 1) offc = base + (hh * 32 + col) * 16;
    else offc = base + 16384 + ((hsd ? 0 : 4) + (hrr - 2) * 2 + hh) * 16;
    if (!hneed) offc = OOB;
    const v4u hcv = ld128c(c.rsW, offc, 0);
#pragma unroll
    for (int i = 0; i < 4; ++i)
      sts128(L, L_RAW + (2 * (2 * p + (i >> 1)) + (lane >> 5)) * PLR + ((lane & 31) + 1) * CSR + ((i & 1) ? 5 : 0) * 16, hr[i]);
    if (lane < 48) sts128(L, hdst, hcv);
  }
  bar_lds();
  produce<1>(c, top, bot, tag);
}

__global__ void __launch_bounds__(512, 2)
resblock_kernel(const u16* __restrict__ x, const u16* __restrict__ temb, const u16* __restrict__ g1,
                const u16* __restrict__ be1, const u16* __restrict__ w1, const u16* __restrict__ wemb,
                const u16* __restrict__ g2, const u16* __restrict__ be2, const u16* __restrict__ w2,
                u16* __restrict__ out, char* ws) {
  extern __shared__ __attribute__((aligned(16))) char smem[];
  Ctx c;
  c.L = (LAS char*)smem;
  c.ws = ws;
  c.tid = threadIdx.x;
  c.lane = c.tid & 63;
  c.w = __builtin_amdgcn_readfirstlane(c.tid >> 6);
  c.wg = blockIdx.x;
  const int xcd = c.wg & 7, idx = c.wg >> 3;
  c.b = xcd >> 2;
  c.ty = (xcd & 3) * 8 + (idx >> 2);
  c.tx = idx & 3;
  c.loc = c.ty * 4 + c.tx;
  c.rsX = __builtin_amdgcn_make_buffer_rsrc((void*)x, 0, X_BYTES, RSRC3);
  c.rsO = __builtin_amdgcn_make_buffer_rsrc((void*)out, 0, X_BYTES, RSRC3);
  c.rsW = __builtin_amdgcn_make_buffer_rsrc((void*)ws, 0, WS_BYTES, RSRC3);
  P1 f;
  phase1_loads(c, (c.loc >> 6) ? w2 : w1, f);
  if (c.w < 4) consumer(c, f);
  else producer(c, f, c.w == 4 ? g1 : c.w == 5 ? be1 : c.w == 6 ? g2 : be2, temb, wemb);
}
}

extern "C" void kernel_launch(void* const* d_in, const int* in_sizes, int n_in, void* d_out, int out_size, void* d_ws,
                              size_t ws_size, hipStream_t stream) {
  (void)in_sizes; (void)n_in; (void)out_size; (void)ws_size;
  (void)hipFuncSetAttribute((const void*)resblock_kernel, hipFuncAttributeMaxDynamicSharedMemorySize, LDS_BYTES);
  resblock_kernel<<<256, 512, LDS_BYTES, stream>>>(
      (const u16*)d_in[0], (const u16*)d_in[1], (const u16*)d_in[2], (const u16*)d_in[3], (const u16*)d_in[4],
      (const u16*)d_in[5], (const u16*)d_in[6], (const u16*)d_in[7], (const u16*)d_in[8], (u16*)d_out, (char*)d_ws);
}
```
